```python
import torch

def _softmax(x, axis=-1):
    return torch.softmax(x, dim=axis)
def _gelu(x):
    return torch.nn.functional.gelu(x, approximate="tanh")
def _silu(x):
    return torch.nn.functional.silu(x)
def _sigmoid(x):
    return torch.sigmoid(x)
def _layernorm(x, axis=-1, eps=1e-5):
    m = x.mean(dim=axis, keepdim=True)
    v = x.var(dim=axis, keepdim=True, unbiased=False)
    return (x - m) / torch.sqrt(v + eps)
def _scatter_add(tgt, idx, src, axis=0):
    return tgt.clone().index_add_(axis, idx, src)
def _segment_sum(src, seg, n):
    out = torch.zeros((n,) + src.shape[1:], dtype=src.dtype)
    return out.index_add_(0, seg, src)
def _pad(x, pw):
    flat = tuple(v for pair in reversed(pw) for v in pair)
    return torch.nn.functional.pad(x, flat)

kernel_name = 'standalone__self_sup_conv_stem_wavlm__speech'

def setup_inputs():

    _w = (lambda a: (lambda t: t.to(torch.bfloat16) if t.is_floating_point() else t)(torch.as_tensor(a)))
    B, T, D, K = 2, 16000, 256, 3
    scale = (K * D) ** -0.5
    return {
        "x": _w((lambda *s: torch.randn(*s, dtype=torch.bfloat16) * 0.05)(B, T, D)),
        "w": _w((lambda *s: torch.randn(*s, dtype=torch.bfloat16) * 0.05)(K, D, D) * scale),
        "gn_gamma": _w((lambda *s: torch.randn(*s, dtype=torch.bfloat16) * 0.05)(D) * 0.1 + 1.0),
        "gn_beta": _w((lambda *s: torch.randn(*s, dtype=torch.bfloat16) * 0.05)(D) * 0.1),
    }

def reference(x, w, gn_gamma, gn_beta):
    B, T, D = x.shape
    K = w.shape[0]
    s = 2
    L = (T - K) // s + 1
    h = torch.einsum('blc,cd->bld', x[:, 0:s * L:s, :], w[0])
    for i in range(1, K):
        h = h + torch.einsum('blc,cd->bld', x[:, i:i + s * L:s, :], w[i])
    mu = torch.einsum('bld->bd', h)[:, None, :] / L
    var = torch.einsum('bld->bd', (h - mu) ** 2)[:, None, :] / L
    h = (h - mu) / torch.sqrt(var + 1e-5)
    h = h * gn_gamma[None, None, :] + gn_beta[None, None, :]
    return _gelu(h)
```

```cpp
#include <hip/hip_runtime.h>
#include <hip/hip_bf16.h>
typedef __attribute__((ext_vector_type(8))) short bf16x8;
typedef __attribute__((ext_vector_type(16))) float f32x16;
constexpr int B_=2,T_=16000,D_=256,L_=7999;

__device__ __forceinline__ float bf2f(unsigned short u){ return __uint_as_float(((unsigned)u)<<16); }

__global__ __launch_bounds__(256) void k_gemm(const unsigned short* __restrict__ x, const unsigned short* __restrict__ w, float* __restrict__ h, float* __restrict__ part){
  int wave = threadIdx.x>>6, lane = threadIdx.x&63;
  int rt = blockIdx.x;
  int b = rt / 250, t = rt % 250;
  int r = lane&31, hh = lane>>5;
  for (int ct = wave; ct < 8; ct += 4) {
    f32x16 acc = {};
    int l = t*32 + r; int lc = l < L_ ? l : L_-1;
    const unsigned short* xa = x + ((size_t)b*T_ + 2*lc)*D_;
    for (int k0 = 0; k0 < 768; k0 += 16) {
      bf16x8 a = *(const bf16x8*)(xa + k0 + 8*hh);
      bf16x8 bb;
      for (int j = 0; j < 8; ++j) bb[j] = (short)w[(size_t)(k0 + 8*hh + j)*D_ + ct*32 + r];
      acc = __builtin_amdgcn_mfma_f32_32x32x16_bf16(a, bb, acc, 0,0,0);
    }
    float s=0.f, q=0.f;
    for (int i = 0; i < 16; ++i) {
      int row = (i&3) + 8*(i>>2) + 4*hh; int ll = t*32+row;
      if (ll < L_) { h[((size_t)b*L_ + ll)*D_ + ct*32 + r] = acc[i]; s += acc[i]; q += acc[i]*acc[i]; }
    }
    s += __shfl_xor(s, 32); q += __shfl_xor(q, 32);
    if (hh == 0) { part[((size_t)rt*D_ + ct*32 + r)*2] = s; part[((size_t)rt*D_ + ct*32 + r)*2+1] = q; }
  }
}
__global__ void k_stats(const float* __restrict__ part, const unsigned short* g, const unsigned short* be, float* __restrict__ sc){
  int b = blockIdx.x, d = threadIdx.x;
  float s=0.f,q=0.f;
  for (int t=0;t<250;++t){ s += part[((size_t)(b*250+t)*D_+d)*2]; q += part[((size_t)(b*250+t)*D_+d)*2+1]; }
  float mu = s/L_; float var = q/L_ - mu*mu; var = var<0?0:var;
  float rs = rsqrtf(var+1e-5f);
  float a = rs*bf2f(g[d]); float c = bf2f(be[d]) - mu*a;
  sc[(b*D_+d)*2]=a; sc[(b*D_+d)*2+1]=c;
}
__global__ void k_norm(const float* __restrict__ h, const float* __restrict__ sc, __hip_bfloat16* __restrict__ out){
  size_t i = (size_t)blockIdx.x*256+threadIdx.x;
  size_t n = (size_t)B_*L_*D_;
  if (i>=n) return;
  int d = i & 255; int b = i / ((size_t)L_*D_);
  float y = h[i]*sc[(b*D_+d)*2]+sc[(b*D_+d)*2+1];
  float u = 0.7978845608028654f*(y+0.044715f*y*y*y);
  float o = 0.5f*y*(1.f+tanhf(u));
  out[i] = __float2bfloat16(o);
}
extern "C" void kernel_launch(void* const* d_in, const int* in_sizes, int n_in, void* d_out, int out_size, void* d_ws, size_t ws_size, hipStream_t stream) {
  const unsigned short* x=(const unsigned short*)d_in[0]; const unsigned short* w=(const unsigned short*)d_in[1];
  const unsigned short* g=(const unsigned short*)d_in[2]; const unsigned short* be=(const unsigned short*)d_in[3];
  float* h=(float*)d_ws; float* part = h + (size_t)B_*L_*D_; float* sc = part + 500*256*2;
  k_gemm<<<500,256,0,stream>>>(x,w,h,part);
  k_stats<<<2,256,0,stream>>>(part,g,be,sc);
  k_norm<<<(B_*L_*D_+255)/256,256,0,stream>>>(h,sc,(__hip_bfloat16*)d_out);
}
```

```cpp
#include <hip/hip_runtime.h>
#include <cstdio>
#include <vector>
#include <algorithm>
typedef unsigned u32;
typedef unsigned long long u64;
typedef u32 u32x2 __attribute__((ext_vector_type(2)));
typedef u32 u32x4 __attribute__((ext_vector_type(4)));
typedef float f32x4 __attribute__((ext_vector_type(4)));
typedef short bf16x8 __attribute__((ext_vector_type(8)));
#define LAS __attribute__((address_space(3)))
#define GAS __attribute__((address_space(1)))

constexpr int T_ = 16000, D_ = 256, L_ = 7999, NT = 125, NWG = 250;
constexpr int CH = 129 * 128;
constexpr int LDS_BYTES = 96 * 1024;
constexpr int AUX_SC1 = 16;
constexpr int WS_NONCE = 0;
constexpr int WS_RES = 4096;
constexpr int WS_SLOT = WS_RES + NWG * 2048;
constexpr int WS_BYTES = WS_SLOT + NWG * 4096;
constexpr u32 SPIN_CAP = 1u << 16;

#define TS(i) do {} while (0)

__device__ __forceinline__ float bf2f(unsigned short u) { return __uint_as_float(((u32)u) << 16); }
__device__ __forceinline__ u32 pk_bf16(float lo, float hi) {
  typedef __bf16 bf16x2 __attribute__((ext_vector_type(2)));
  typedef float f32x2 __attribute__((ext_vector_type(2)));
  f32x2 v = {lo, hi};
  bf16x2 r = __builtin_convertvector(v, bf16x2);
  return __builtin_bit_cast(u32, r);
}
__device__ __forceinline__ float gelu_tanh(float y) {
  const float c0 = -2.0f * 0.7978845608028654f * 1.4426950408889634f;
  const float c1 = c0 * 0.044715f;
  float t = __builtin_fmaf(y * y, c1, c0);
  float e = __builtin_amdgcn_exp2f(y * t);
  return y * __builtin_amdgcn_rcpf(1.0f + e);
}
__device__ __forceinline__ float add_x32(float a, float b) {
  auto r = __builtin_amdgcn_permlane32_swap(__float_as_uint(a), __float_as_uint(b), false, false);
  return __uint_as_float(r[0]) + __uint_as_float(r[1]);
}
__device__ __forceinline__ float add_x16(float a, float b) {
  auto r = __builtin_amdgcn_permlane16_swap(__float_as_uint(a), __float_as_uint(b), false, false);
  return __uint_as_float(r[0]) + __uint_as_float(r[1]);
}

__global__ __launch_bounds__(320, 2) void k_fused(const unsigned short* __restrict__ x, const unsigned short* __restrict__ w,
                                                  const unsigned short* __restrict__ gamma, const unsigned short* __restrict__ beta,
                                                  unsigned short* __restrict__ out, unsigned char* __restrict__ ws) {
  __shared__ __attribute__((aligned(16))) unsigned char smem[LDS_BYTES];
  const int g = blockIdx.x, b = g >= NT ? 1 : 0, tile = g - b * NT;
  const int wave = __builtin_amdgcn_readfirstlane(threadIdx.x >> 6), lane = threadIdx.x & 63;
  const int rot = g & 3;
  const __amdgpu_buffer_rsrc_t rsW = __builtin_amdgcn_make_buffer_rsrc((void*)ws, 0, WS_BYTES, 0x00020000);
  TS(0);
  if (wave == 0) {
    const __amdgpu_buffer_rsrc_t rsX = __builtin_amdgcn_make_buffer_rsrc((void*)x, 0, 2 * T_ * D_ * 2, 0x00020000);
    const int lr = lane >> 3;
    const int v0 = lr * 1024 + (((lane & 7) ^ (lr >> 1)) << 4), v1 = v0 ^ 64;
    const int xbase = (b * T_ + tile * 128) * 512;
#pragma unroll
    for (int ci = 0; ci < 4; ++ci) {
      const int cb = xbase + (((ci + rot) & 3) << 7);
#pragma unroll
      for (int q = 0; q < 16; ++q)
        __builtin_amdgcn_raw_ptr_buffer_load_lds(rsX, (LAS void*)(smem + ci * CH + q * 1024), 16, (q & 1) ? v1 : v0,
                                                 cb + (q & 7) * 8192 + (q < 8 ? 512 : 0), 0, 2);
      if (lane < 8) __builtin_amdgcn_raw_ptr_buffer_load_lds(rsX, (LAS void*)(smem + ci * CH + 16384), 16, v0, cb + 65536, 0, 2);
    }
    TS(1);
    asm volatile("s_waitcnt vmcnt(51)" ::: "memory"); TS(2); __builtin_amdgcn_s_barrier();
    asm volatile("s_waitcnt vmcnt(34)" ::: "memory"); TS(3); __builtin_amdgcn_s_barrier();
    asm volatile("s_waitcnt vmcnt(17)" ::: "memory"); TS(4); __builtin_amdgcn_s_barrier();
    asm volatile("s_waitcnt vmcnt(0)" ::: "memory"); TS(5); __builtin_amdgcn_s_barrier();
    float gm = 0.f, bt = 0.f; u32 nonce = 0;
    if (tile < 32) {
      gm = bf2f(gamma[tile * 8 + (lane & 7)]); bt = bf2f(beta[tile * 8 + (lane & 7)]);
      nonce = *(LAS u32*)(smem + 4 * CH);
    }
    __builtin_amdgcn_s_barrier();
    TS(6);
    if (tile < 32) {
      const int off0 = WS_SLOT + (b * NT + lr) * 4096 + tile * 128 + (lane & 7) * 16;
      float s, q;
      for (u32 spins = 0;; ++spins) {
        u32x4 v[16];
        asm volatile("" ::: "memory");
#pragma unroll
        for (int i = 0; i < 16; ++i) v[i] = __builtin_amdgcn_raw_buffer_load_b128(rsW, off0 + (i == 15 && lr >= 5 ? 0 : i * 32768), 0, AUX_SC1);
        bool ok = true; s = 0.f; q = 0.f;
#pragma unroll
        for (int i = 0; i < 16; ++i) {
          ok &= (v[i].y == nonce) & (v[i].w == nonce);
          const bool use = !(i == 15 && lr >= 5);
          s += use ? __uint_as_float(v[i].x) : 0.f; q += use ? __uint_as_float(v[i].z) : 0.f;
        }
        if (__all(ok) || spins > SPIN_CAP) break;
        __builtin_amdgcn_s_sleep(1);
      }
      TS(7);
      s += __shfl_xor(s, 8); q += __shfl_xor(q, 8);
      s = add_x16(s, s); q = add_x16(q, q);
      s = add_x32(s, s); q = add_x32(q, q);
      const float mean = s * (1.0f / L_);
      float var = q * (1.0f / L_) - mean * mean; var = var < 0.f ? 0.f : var;
      const float a = gm * __builtin_amdgcn_rsqf(var + 1e-5f), c = bt - mean * a;
      u32 ua = __float_as_uint(a), uc = __float_as_uint(c);
      ua = ua == 0xFFFFFFFFu ? 0x7FC00000u : ua; uc = uc == 0xFFFFFFFFu ? 0x7FC00000u : uc;
      const int src = (lane & 3) * 2;
      u32x4 o; o.x = __shfl(ua, src); o.y = __shfl(uc, src); o.z = __shfl(ua, src + 1); o.w = __shfl(uc, src + 1);
      const int roff = WS_RES + (b * NT + (lane >> 2)) * 2048 + tile * 64 + (lane & 3) * 16;
#pragma unroll
      for (int i = 0; i < 8; ++i)
        if (i < 7 || (lane >> 2) < 13) __builtin_amdgcn_raw_buffer_store_b128(o, rsW, roff + i * 32768, 0, AUX_SC1);
      TS(8);
    }
  } else {
    const int wc = wave - 1, n = lane & 15, q4 = lane >> 4;
    const __amdgpu_buffer_rsrc_t rsB = __builtin_amdgcn_make_buffer_rsrc((void*)w, 0, 768 * 512, 0x00020000);
    if (lane < 32) __builtin_amdgcn_raw_buffer_store_b128((u32x4){~0u, ~0u, ~0u, ~0u}, rsW, WS_RES + g * 2048 + wc * 512 + lane * 16, 0, AUX_SC1);
    u32 old = 0;
    if (tile < 32) {
#pragma unroll
      for (int i = 0; i < 4; ++i) {
        const int p = (wc * 4 + i) * 8 + (lane >> 3);
        if (p < NT) __builtin_amdgcn_raw_buffer_store_b128((u32x4){0u, 0u, 0u, 0u}, rsW, WS_SLOT + (b * NT + p) * 4096 + tile * 128 + (lane & 7) * 16, 0, AUX_SC1);
      }
      old = __builtin_amdgcn_raw_buffer_load_b32(rsW, WS_NONCE + (b * 32 + tile) * 4, 0, AUX_SC1);
    }
    __builtin_amdgcn_sched_barrier(0);
    const int wv = q4 * 4096 + wc * 128 + n * 8;
    u32x2 raw[6][8];
#pragma unroll
    for (int s = 0; s < 6; ++s)
#pragma unroll
      for (int j = 0; j < 8; ++j)
        raw[s][j] = __builtin_amdgcn_raw_buffer_load_b64(rsB, wv + j * 512, ((s >> 1) * 256 + rot * 64 + (s & 1) * 32) * 512, 0);
    __builtin_amdgcn_sched_barrier(0);
    TS(1);
    const int pr = n < 4 ? 2 * n : (n < 12 ? 2 * n - 7 : 2 * n - 16);
    int aoff[2][2];
#pragma unroll
    for (int sh = 0; sh < 2; ++sh)
#pragma unroll
      for (int ks = 0; ks < 2; ++ks) { const int j = pr + sh; aoff[sh][ks] = j * 128 + (((4 * ks + q4) ^ ((j >> 1) & 7)) << 4); }
    f32x4 acc[4][4];
#pragma unroll
    for (int i = 0; i < 4; ++i)
#pragma unroll
      for (int t = 0; t < 4; ++t) acc[i][t] = (f32x4){0.f, 0.f, 0.f, 0.f};
    asm volatile("s_waitcnt vmcnt(48)" ::: "memory");
    u32 nonce_in = 0;
    for (int ci = 0; ci < 4; ++ci) {
      asm volatile("" ::: "memory"); __builtin_amdgcn_s_barrier(); asm volatile("" ::: "memory");
      if (ci == 0 && tile < 32 && wc == 0) {
        u32 nn = old + 1u; nn = nn ? nn : 1u;
        if (lane == 0) { __builtin_amdgcn_raw_buffer_store_b32(nn, rsW, WS_NONCE + (b * 32 + tile) * 4, 0, AUX_SC1); *(LAS u32*)(smem + 4 * CH) = nn; }
      }
      if (ci == 3) nonce_in = __builtin_amdgcn_raw_buffer_load_b32(rsW, WS_NONCE + (b * 32 + wc * 8 + (n >> 1)) * 4, 0, AUX_SC1);
      const int nxt = ci < 3 ? ((ci + 1 + rot) & 3) * 64 * 512 : 0x7FFF0000 - 0x60000;
      const LAS unsigned char* cb = (const LAS unsigned char*)smem + ci * CH;
#pragma unroll
      for (int s = 0; s < 6; ++s) {
        const int tap = s >> 1, ks = s & 1;
        bf16x8 af[4];
#pragma unroll
        for (int rt = 0; rt < 4; ++rt) af[rt] = *(const LAS bf16x8*)(cb + aoff[tap == 2][ks] + rt * 2048 + (tap == 1 ? 0 : 8192));
        u32x4 bfr[4];
#pragma unroll
        for (int p = 0; p < 4; ++p) {
          bfr[0][p] = __builtin_amdgcn_perm(raw[s][2 * p + 1].x, raw[s][2 * p].x, 0x05040100u);
          bfr[1][p] = __builtin_amdgcn_perm(raw[s][2 * p + 1].x, raw[s][2 * p].x, 0x07060302u);
          bfr[2][p] = __builtin_amdgcn_perm(raw[s][2 * p + 1].y, raw[s][2 * p].y, 0x05040100u);
          bfr[3][p] = __builtin_amdgcn_perm(raw[s][2 * p + 1].y, raw[s][2 * p].y, 0x07060302u);
        }
#pragma unroll
        for (int j = 0; j < 8; ++j)
          raw[s][j] = __builtin_amdgcn_raw_buffer_load_b64(rsB, wv + j * 512, nxt + (tap * 256 + ks * 32) * 512, 0);
#pragma unroll
        for (int rt = 0; rt < 4; ++rt)
#pragma unroll
          for (int t = 0; t < 4; ++t)
            acc[rt][t] = __builtin_amdgcn_mfma_f32_16x16x32_bf16(af[rt], __builtin_bit_cast(bf16x8, bfr[t]), acc[rt][t], 0, 0, 0);
        __builtin_amdgcn_sched_barrier(0);
      }
    }
    TS(6);
    if (tile == NT - 1 && q4 == 2) {
#pragma unroll
      for (int t = 0; t < 4; ++t) acc[3][t][3] = 0.f;
    }
    float sm[4], sq[4];
#pragma unroll
    for (int t = 0; t < 4; ++t) {
      float s = 0.f, q = 0.f;
#pragma unroll
      for (int rt = 0; rt < 4; ++rt)
#pragma unroll
        for (int r = 0; r < 4; ++r) { const float v = acc[rt][t][r]; s += v; q = __builtin_fmaf(v, v, q); }
      sm[t] = s; sq[t] = q;
    }
    const float S = add_x16(add_x32(sm[0], sm[2]), add_x32(sm[1], sm[3]));
    const float Q = add_x16(add_x32(sq[0], sq[2]), add_x32(sq[1], sq[3]));
    const int soff = WS_SLOT + g * 4096 + (wc * 64 + n * 4 + q4) * 16;
    __builtin_amdgcn_raw_buffer_store_b128((u32x4){__float_as_uint(S), nonce_in, __float_as_uint(Q), nonce_in}, rsW, soff, 0, AUX_SC1);
    TS(7);
    __builtin_amdgcn_s_barrier();
    u32x4 r0, r1;
    const int roff = WS_RES + g * 2048 + wc * 512 + n * 32;
    for (u32 spins = 0;; ++spins) {
      asm volatile("" ::: "memory");
      r0 = __builtin_amdgcn_raw_buffer_load_b128(rsW, roff, 0, AUX_SC1);
      r1 = __builtin_amdgcn_raw_buffer_load_b128(rsW, roff + 16, 0, AUX_SC1);
      const bool ok = (r0.x != ~0u) & (r0.y != ~0u) & (r0.z != ~0u) & (r0.w != ~0u) & (r1.x != ~0u) & (r1.y != ~0u) & (r1.z != ~0u) & (r1.w != ~0u);
      if (__all(ok) || spins > SPIN_CAP) break;
      if ((spins & 15u) == 15u) {
        const u32 nn = __builtin_amdgcn_raw_buffer_load_b32(rsW, WS_NONCE + (b * 32 + wc * 8 + (n >> 1)) * 4, 0, AUX_SC1);
        if (nn != nonce_in) { nonce_in = nn; __builtin_amdgcn_raw_buffer_store_b128((u32x4){__float_as_uint(S), nn, __float_as_uint(Q), nn}, rsW, soff, 0, AUX_SC1); }
      }
      __builtin_amdgcn_s_sleep(1);
    }
    TS(8);
    const float a0 = __uint_as_float(r0.x), c0 = __uint_as_float(r0.y), a1 = __uint_as_float(r0.z), c1 = __uint_as_float(r0.w);
    const float a2 = __uint_as_float(r1.x), c2 = __uint_as_float(r1.y), a3 = __uint_as_float(r1.z), c3 = __uint_as_float(r1.w);
    const __amdgpu_buffer_rsrc_t rsO = __builtin_amdgcn_make_buffer_rsrc((void*)(out + (size_t)b * L_ * D_), 0, L_ * D_ * 2, 0x00020000);
    const int rb = q4 == 0 ? 0 : (q4 == 1 ? 1 : (q4 == 2 ? 9 : 8));
    const int ov = rb * 512 + wc * 128 + n * 8;
#pragma unroll
    for (int rt = 0; rt < 4; ++rt)
#pragma unroll
      for (int r = 0; r < 4; ++r) {
        u32x2 o;
        o.x = pk_bf16(gelu_tanh(__builtin_fmaf(acc[rt][0][r], a0, c0)), gelu_tanh(__builtin_fmaf(acc[rt][1][r], a1, c1)));
        o.y = pk_bf16(gelu_tanh(__builtin_fmaf(acc[rt][2][r], a2, c2)), gelu_tanh(__builtin_fmaf(acc[rt][3][r], a3, c3)));
        __builtin_amdgcn_raw_buffer_store_b64(o, rsO, ov + r * 1024, (tile * 64 + rt * 16) * 512, 16);
      }
    TS(9);
    TS(10);
  }
}

extern "C" void kernel_launch(void* const* d_in, const int* in_sizes, int n_in, void* d_out, int out_size, void* d_ws, size_t ws_size, hipStream_t stream) {
  k_fused<<<NWG, 320, 0, stream>>>((const unsigned short*)d_in[0], (const unsigned short*)d_in[1], (const unsigned short*)d_in[2],
                                   (const unsigned short*)d_in[3], (unsigned short*)d_out, (unsigned char*)d_ws);
}
```

```cpp
#define AMP 0
#define PA 0
#define PB 10
#include <hip/hip_runtime.h>
#include <cstdio>
#define STAMP 1
#include <vector>
#include <algorithm>
typedef unsigned u32;
typedef unsigned long long u64;
typedef u32 u32x2 __attribute__((ext_vector_type(2)));
typedef u32 u32x4 __attribute__((ext_vector_type(4)));
typedef float f32x4 __attribute__((ext_vector_type(4)));
typedef short bf16x8 __attribute__((ext_vector_type(8)));
#define LAS __attribute__((address_space(3)))
#define GAS __attribute__((address_space(1)))

constexpr int T_ = 16000, D_ = 256, L_ = 7999, NT = 125, NWG = 250;
constexpr int CH = 129 * 128;
constexpr int LDS_BYTES = 96 * 1024;
constexpr int AUX_SC1 = 16;
constexpr int WS_NONCE = 0;
constexpr int WS_RES = 4096;
constexpr int WS_SLOT = WS_RES + NWG * 2048;
constexpr int WS_BYTES = WS_SLOT + NWG * 4096;
constexpr u32 SPIN_CAP = 1u << 16;

#define TS(i) do { __builtin_amdgcn_sched_barrier(0); ts[i] = __builtin_amdgcn_s_memrealtime(); __builtin_amdgcn_sched_barrier(0); } while (0)

__device__ __forceinline__ float bf2f(unsigned short u) { return __uint_as_float(((u32)u) << 16); }
__device__ __forceinline__ u32 pk_bf16(float lo, float hi) {
  typedef __bf16 bf16x2 __attribute__((ext_vector_type(2)));
  typedef float f32x2 __attribute__((ext_vector_type(2)));
  f32x2 v = {lo, hi};
  bf16x2 r = __builtin_convertvector(v, bf16x2);
  return __builtin_bit_cast(u32, r);
}
__device__ __forceinline__ float gelu_tanh(float y) {
  const float c0 = -2.0f * 0.7978845608028654f * 1.4426950408889634f;
  const float c1 = c0 * 0.044715f;
  float t = __builtin_fmaf(y * y, c1, c0);
  float e = __builtin_amdgcn_exp2f(y * t);
  return y * __builtin_amdgcn_rcpf(1.0f + e);
}
__device__ __forceinline__ float add_x32(float a, float b) {
  auto r = __builtin_amdgcn_permlane32_swap(__float_as_uint(a), __float_as_uint(b), false, false);
  return __uint_as_float(r[0]) + __uint_as_float(r[1]);
}
__device__ __forceinline__ float add_x16(float a, float b) {
  auto r = __builtin_amdgcn_permlane16_swap(__float_as_uint(a), __float_as_uint(b), false, false);
  return __uint_as_float(r[0]) + __uint_as_float(r[1]);
}

__global__ __launch_bounds__(320, 2) void k_fused(const unsigned short* __restrict__ x, const unsigned short* __restrict__ w,
                                                  const unsigned short* __restrict__ gamma, const unsigned short* __restrict__ beta,
                                                  unsigned short* __restrict__ out, unsigned char* __restrict__ ws) {
  __shared__ __attribute__((aligned(16))) unsigned char smem[LDS_BYTES];
  const int g = blockIdx.x, b = g >= NT ? 1 : 0, tile = g - b * NT;
  const int wave = __builtin_amdgcn_readfirstlane(threadIdx.x >> 6), lane = threadIdx.x & 63;
  const int rot = g & 3;
  const __amdgpu_buffer_rsrc_t rsW = __builtin_amdgcn_make_buffer_rsrc((void*)ws, 0, WS_BYTES, 0x00020000);
  u64 ts[12];
  for (int i = 0; i < 12; ++i) ts[i] = 0;
  TS(0);
  if (wave == 0) {
    const __amdgpu_buffer_rsrc_t rsX = __builtin_amdgcn_make_buffer_rsrc((void*)x, 0, 2 * T_ * D_ * 2, 0x00020000);
    const int lr = lane >> 3;
    const int v0 = lr * 1024 + (((lane & 7) ^ (lr >> 1)) << 4), v1 = v0 ^ 64;
    const int xbase = (b * T_ + tile * 128) * 512;
#pragma unroll
    for (int ci = 0; ci < 4; ++ci) {
      const int cb = xbase + (((ci + rot) & 3) << 7);
#pragma unroll
      for (int q = 0; q < 16; ++q)
        __builtin_amdgcn_raw_ptr_buffer_load_lds(rsX, (LAS void*)(smem + ci * CH + q * 1024), 16, (q & 1) ? v1 : v0,
                                                 cb + (q & 7) * 8192 + (q < 8 ? 512 : 0), 0, 2);
      if (lane < 8) __builtin_amdgcn_raw_ptr_buffer_load_lds(rsX, (LAS void*)(smem + ci * CH + 16384), 16, v0, cb + 65536, 0, 2);
    }
    TS(1);
    asm volatile("s_waitcnt vmcnt(51)" ::: "memory"); TS(2); __builtin_amdgcn_s_barrier();
    asm volatile("s_waitcnt vmcnt(34)" ::: "memory"); TS(3); __builtin_amdgcn_s_barrier();
    asm volatile("s_waitcnt vmcnt(17)" ::: "memory"); TS(4); __builtin_amdgcn_s_barrier();
    asm volatile("s_waitcnt vmcnt(0)" ::: "memory"); TS(5); __builtin_amdgcn_s_barrier();
    float gm = 0.f, bt = 0.f; u32 nonce = 0;
    if (tile < 32) {
      gm = bf2f(gamma[tile * 8 + (lane & 7)]); bt = bf2f(beta[tile * 8 + (lane & 7)]);
      nonce = *(LAS u32*)(smem + 4 * CH);
    }
    __builtin_amdgcn_s_barrier();
    TS(6);
    if (tile < 32) {
      const int off0 = WS_SLOT + (b * NT + lr) * 4096 + tile * 128 + (lane & 7) * 16;
      float s, q;
      for (u32 spins = 0;; ++spins) {
        u32x4 v[16];
        asm volatile("" ::: "memory");
#pragma unroll
        for (int i = 0; i < 16; ++i) v[i] = __builtin_amdgcn_raw_buffer_load_b128(rsW, off0 + (i == 15 && lr >= 5 ? 0 : i * 32768), 0, AUX_SC1);
        bool ok = true; s = 0.f; q = 0.f;
#pragma unroll
        for (int i = 0; i < 16; ++i) {
          ok &= (v[i].y == nonce) & (v[i].w == nonce);
          const bool use = !(i == 15 && lr >= 5);
          s += use ? __uint_as_float(v[i].x) : 0.f; q += use ? __uint_as_float(v[i].z) : 0.f;
        }
        if (__all(ok) || spins > SPIN_CAP) break;
        __builtin_amdgcn_s_sleep(1);
      }
      TS(7);
      s += __shfl_xor(s, 8); q += __shfl_xor(q, 8);
      s = add_x16(s, s); q = add_x16(q, q);
      s = add_x32(s, s); q = add_x32(q, q);
      const float mean = s * (1.0f / L_);
      float var = q * (1.0f / L_) - mean * mean; var = var < 0.f ? 0.f : var;
      const float a = gm * __builtin_amdgcn_rsqf(var + 1e-5f), c = bt - mean * a;
      u32 ua = __float_as_uint(a), uc = __float_as_uint(c);
      ua = ua == 0xFFFFFFFFu ? 0x7FC00000u : ua; uc = uc == 0xFFFFFFFFu ? 0x7FC00000u : uc;
      const int src = (lane & 3) * 2;
      u32x4 o; o.x = __shfl(ua, src); o.y = __shfl(uc, src); o.z = __shfl(ua, src + 1); o.w = __shfl(uc, src + 1);
      const int roff = WS_RES + (b * NT + (lane >> 2)) * 2048 + tile * 64 + (lane & 3) * 16;
#pragma unroll
      for (int i = 0; i < 8; ++i)
        if (i < 7 || (lane >> 2) < 13) __builtin_amdgcn_raw_buffer_store_b128(o, rsW, roff + i * 32768, 0, AUX_SC1);
      TS(8);
    }
  } else {
    const int wc = wave - 1, n = lane & 15, q4 = lane >> 4;
    const __amdgpu_buffer_rsrc_t rsB = __builtin_amdgcn_make_buffer_rsrc((void*)w, 0, 768 * 512, 0x00020000);
    if (lane < 32) __builtin_amdgcn_raw_buffer_store_b128((u32x4){~0u, ~0u, ~0u, ~0u}, rsW, WS_RES + g * 2048 + wc * 512 + lane * 16, 0, AUX_SC1);
    u32 old = 0;
    if (tile < 32) {
#pragma unroll
      for (int i = 0; i < 4; ++i) {
        const int p = (wc * 4 + i) * 8 + (lane >> 3);
        if (p < NT) __builtin_amdgcn_raw_buffer_store_b128((u32x4){0u, 0u, 0u, 0u}, rsW, WS_SLOT + (b * NT + p) * 4096 + tile * 128 + (lane & 7) * 16, 0, AUX_SC1);
      }
      old = __builtin_amdgcn_raw_buffer_load_b32(rsW, WS_NONCE + (b * 32 + tile) * 4, 0, AUX_SC1);
    }
    __builtin_amdgcn_sched_barrier(0);
    const int wv = q4 * 4096 + wc * 128 + n * 8;
    u32x2 raw[6][8];
#pragma unroll
    for (int s = 0; s < 6; ++s)
#pragma unroll
      for (int j = 0; j < 8; ++j)
        raw[s][j] = __builtin_amdgcn_raw_buffer_load_b64(rsB, wv + j * 512, ((s >> 1) * 256 + rot * 64 + (s & 1) * 32) * 512, 0);
    __builtin_amdgcn_sched_barrier(0);
    TS(1);
    const int pr = n < 4 ? 2 * n : (n < 12 ? 2 * n - 7 : 2 * n - 16);
    int aoff[2][2];
#pragma unroll
    for (int sh = 0; sh < 2; ++sh)
#pragma unroll
      for (int ks = 0; ks < 2; ++ks) { const int j = pr + sh; aoff[sh][ks] = j * 128 + (((4 * ks + q4) ^ ((j >> 1) & 7)) << 4); }
    f32x4 acc[4][4];
#pragma unroll
    for (int i = 0; i < 4; ++i)
#pragma unroll
      for (int t = 0; t < 4; ++t) acc[i][t] = (f32x4){0.f, 0.f, 0.f, 0.f};
    asm volatile("s_waitcnt vmcnt(48)" ::: "memory");
    u32 nonce_in = 0;
    for (int ci = 0; ci < 4; ++ci) {
      asm volatile("" ::: "memory"); __builtin_amdgcn_s_barrier(); asm volatile("" ::: "memory");
      if (ci == 0) TS(2); else if (ci == 1) TS(3); else if (ci == 2) TS(4); else TS(5);
      if (ci == 0 && tile < 32 && wc == 0) {
        u32 nn = old + 1u; nn = nn ? nn : 1u;
        if (lane == 0) { __builtin_amdgcn_raw_buffer_store_b32(nn, rsW, WS_NONCE + (b * 32 + tile) * 4, 0, AUX_SC1); *(LAS u32*)(smem + 4 * CH) = nn; }
      }
      if (ci == 3) nonce_in = __builtin_amdgcn_raw_buffer_load_b32(rsW, WS_NONCE + (b * 32 + wc * 8 + (n >> 1)) * 4, 0, AUX_SC1);
      const int nxt = ci < 3 ? ((ci + 1 + rot) & 3) * 64 * 512 : 0x7FFF0000 - 0x60000;
      const LAS unsigned char* cb = (const LAS unsigned char*)smem + ci * CH;
#pragma unroll
      for (int s = 0; s < 6; ++s) {
        const int tap = s >> 1, ks = s & 1;
        bf16x8 af[4];
#pragma unroll
        for (int rt = 0; rt < 4; ++rt) af[rt] = *(const LAS bf16x8*)(cb + aoff[tap == 2][ks] + rt * 2048 + (tap == 1 ? 0 : 8192));
        u32x4 bfr[4];
#pragma unroll
        for (int p = 0; p < 4; ++p) {
          bfr[0][p] = __builtin_amdgcn_perm(raw[s][2 * p + 1].x, raw[s][2 * p].x, 0x05040100u);
          bfr[1][p] = __builtin_amdgcn_perm(raw[s][2 * p + 1].x, raw[s][2 * p].x, 0x07060302u);
          bfr[2][p] = __builtin_amdgcn_perm(raw[s][2 * p + 1].y, raw[s][2 * p].y, 0x05040100u);
          bfr[3][p] = __builtin_amdgcn_perm(raw[s][2 * p + 1].y, raw[s][2 * p].y, 0x07060302u);
        }
#pragma unroll
        for (int j = 0; j < 8; ++j)
          raw[s][j] = __builtin_amdgcn_raw_buffer_load_b64(rsB, wv + j * 512, nxt + (tap * 256 + ks * 32) * 512, 0);
#pragma unroll
        for (int rt = 0; rt < 4; ++rt)
#pragma unroll
          for (int t = 0; t < 4; ++t)
            acc[rt][t] = __builtin_amdgcn_mfma_f32_16x16x32_bf16(af[rt], __builtin_bit_cast(bf16x8, bfr[t]), acc[rt][t], 0, 0, 0);
        __builtin_amdgcn_sched_barrier(0);
      }
    }
    TS(6);
    if (tile == NT - 1 && q4 == 2) {
#pragma unroll
      for (int t = 0; t < 4; ++t) acc[3][t][3] = 0.f;
    }
    float sm[4], sq[4];
#pragma unroll
    for (int t = 0; t < 4; ++t) {
      float s = 0.f, q = 0.f;
#pragma unroll
      for (int rt = 0; rt < 4; ++rt)
#pragma unroll
        for (int r = 0; r < 4; ++r) { const float v = acc[rt][t][r]; s += v; q = __builtin_fmaf(v, v, q); }
      sm[t] = s; sq[t] = q;
    }
    const float S = add_x16(add_x32(sm[0], sm[2]), add_x32(sm[1], sm[3]));
    const float Q = add_x16(add_x32(sq[0], sq[2]), add_x32(sq[1], sq[3]));
    const int soff = WS_SLOT + g * 4096 + (wc * 64 + n * 4 + q4) * 16;
    __builtin_amdgcn_raw_buffer_store_b128((u32x4){__float_as_uint(S), nonce_in, __float_as_uint(Q), nonce_in}, rsW, soff, 0, AUX_SC1);
    TS(7);
    __builtin_amdgcn_s_barrier();
    u32x4 r0, r1;
    const int roff = WS_RES + g * 2048 + wc * 512 + n * 32;
    for (u32 spins = 0;; ++spins) {
      asm volatile("" ::: "memory");
      r0 = __builtin_amdgcn_raw_buffer_load_b128(rsW, roff, 0, AUX_SC1);
      r1 = __builtin_amdgcn_raw_buffer_load_b128(rsW, roff + 16, 0, AUX_SC1);
      const bool ok = (r0.x != ~0u) & (r0.y != ~0u) & (r0.z != ~0u) & (r0.w != ~0u) & (r1.x != ~0u) & (r1.y != ~0u) & (r1.z != ~0u) & (r1.w != ~0u);
      if (__all(ok) || spins > SPIN_CAP) break;
      if ((spins & 15u) == 15u) {
        const u32 nn = __builtin_amdgcn_raw_buffer_load_b32(rsW, WS_NONCE + (b * 32 + wc * 8 + (n >> 1)) * 4, 0, AUX_SC1);
        if (nn != nonce_in) { nonce_in = nn; __builtin_amdgcn_raw_buffer_store_b128((u32x4){__float_as_uint(S), nn, __float_as_uint(Q), nn}, rsW, soff, 0, AUX_SC1); }
      }
      __builtin_amdgcn_s_sleep(1);
    }
    TS(8);
    const float a0 = __uint_as_float(r0.x), c0 = __uint_as_float(r0.y), a1 = __uint_as_float(r0.z), c1 = __uint_as_float(r0.w);
    const float a2 = __uint_as_float(r1.x), c2 = __uint_as_float(r1.y), a3 = __uint_as_float(r1.z), c3 = __uint_as_float(r1.w);
    const __amdgpu_buffer_rsrc_t rsO = __builtin_amdgcn_make_buffer_rsrc((void*)(out + (size_t)b * L_ * D_), 0, L_ * D_ * 2, 0x00020000);
    const int rb = q4 == 0 ? 0 : (q4 == 1 ? 1 : (q4 == 2 ? 9 : 8));
    const int ov = rb * 512 + wc * 128 + n * 8;
#pragma unroll
    for (int rt = 0; rt < 4; ++rt)
#pragma unroll
      for (int r = 0; r < 4; ++r) {
        u32x2 o;
        o.x = pk_bf16(gelu_tanh(__builtin_fmaf(acc[rt][0][r], a0, c0)), gelu_tanh(__builtin_fmaf(acc[rt][1][r], a1, c1)));
        o.y = pk_bf16(gelu_tanh(__builtin_fmaf(acc[rt][2][r], a2, c2)), gelu_tanh(__builtin_fmaf(acc[rt][3][r], a3, c3)));
        __builtin_amdgcn_raw_buffer_store_b64(o, rsO, ov + r * 1024, (tile * 64 + rt * 16) * 512, 16);
      }
    TS(9);
    asm volatile("s_waitcnt vmcnt(0)" ::: "memory");
    TS(10);
  }
  if (wave > 0) { const u64 tend = __builtin_amdgcn_s_memrealtime() + AMP * (ts[PB] - ts[PA]); u32 sp = 0; while (__builtin_amdgcn_s_memrealtime() < tend && ++sp < 1000000) __builtin_amdgcn_s_sleep(2); }
}

extern "C" void kernel_launch(void* const* d_in, const int* in_sizes, int n_in, void* d_out, int out_size, void* d_ws, size_t ws_size, hipStream_t stream) {
  k_fused<<<NWG, 320, 0, stream>>>((const unsigned short*)d_in[0], (const unsigned short*)d_in[1], (const unsigned short*)d_in[2],
                                   (const unsigned short*)d_in[3], (unsigned short*)d_out, (unsigned char*)d_ws);
}
extern "C" void debug_report(const void* p, size_t n) {
  const u64* d = (const u64*)p;
  u64 t0 = ~0ull;
  for (int g = 0; g < NWG; ++g) for (int w = 0; w < 5; ++w) t0 = std::min(t0, d[(size_t)(g * 8 + w) * 16]);
  for (int cls = 0; cls < 2; ++cls) {
    printf(cls ? "mfma waves:\n" : "helper wave:\n");
    for (int i = 0; i < 12; ++i) {
      std::vector<double> v;
      for (int g = 0; g < NWG; ++g) for (int w = cls ? 1 : 0; w < (cls ? 5 : 1); ++w) { u64 t = d[(size_t)(g * 8 + w) * 16 + i]; if (t) v.push_back((double)(t - t0) / 100.0); }
      if (v.empty()) continue;
      std::sort(v.begin(), v.end());
      printf("  ts%-2d n=%4zu min %6.2f p10 %6.2f med %6.2f p90 %6.2f max %6.2f\n", i, v.size(), v[0], v[v.size() / 10], v[v.size() / 2], v[v.size() * 9 / 10], v.back());
    }
  }
}
```

```cpp
#define AMP 8
#define PA 0
#define PB 10
#include <hip/hip_runtime.h>
#include <cstdio>
#define STAMP 1
#include <vector>
#include <algorithm>
typedef unsigned u32;
typedef unsigned long long u64;
typedef u32 u32x2 __attribute__((ext_vector_type(2)));
typedef u32 u32x4 __attribute__((ext_vector_type(4)));
typedef float f32x4 __attribute__((ext_vector_type(4)));
typedef short bf16x8 __attribute__((ext_vector_type(8)));
#define LAS __attribute__((address_space(3)))
#define GAS __attribute__((address_space(1)))

constexpr int T_ = 16000, D_ = 256, L_ = 7999, NT = 125, NWG = 250;
constexpr int CH = 129 * 128;
constexpr int LDS_BYTES = 96 * 1024;
constexpr int AUX_SC1 = 16;
constexpr int WS_NONCE = 0;
constexpr int WS_RES = 4096;
constexpr int WS_SLOT = WS_RES + NWG * 2048;
constexpr int WS_BYTES = WS_SLOT + NWG * 4096;
constexpr u32 SPIN_CAP = 1u << 16;

#define TS(i) do { __builtin_amdgcn_sched_barrier(0); ts[i] = __builtin_amdgcn_s_memrealtime(); __builtin_amdgcn_sched_barrier(0); } while (0)

__device__ __forceinline__ float bf2f(unsigned short u) { return __uint_as_float(((u32)u) << 16); }
__device__ __forceinline__ u32 pk_bf16(float lo, float hi) {
  typedef __bf16 bf16x2 __attribute__((ext_vector_type(2)));
  typedef float f32x2 __attribute__((ext_vector_type(2)));
  f32x2 v = {lo, hi};
  bf16x2 r = __builtin_convertvector(v, bf16x2);
  return __builtin_bit_cast(u32, r);
}
__device__ __forceinline__ float gelu_tanh(float y) {
  const float c0 = -2.0f * 0.7978845608028654f * 1.4426950408889634f;
  const float c1 = c0 * 0.044715f;
  float t = __builtin_fmaf(y * y, c1, c0);
  float e = __builtin_amdgcn_exp2f(y * t);
  return y * __builtin_amdgcn_rcpf(1.0f + e);
}
__device__ __forceinline__ float add_x32(float a, float b) {
  auto r = __builtin_amdgcn_permlane32_swap(__float_as_uint(a), __float_as_uint(b), false, false);
  return __uint_as_float(r[0]) + __uint_as_float(r[1]);
}
__device__ __forceinline__ float add_x16(float a, float b) {
  auto r = __builtin_amdgcn_permlane16_swap(__float_as_uint(a), __float_as_uint(b), false, false);
  return __uint_as_float(r[0]) + __uint_as_float(r[1]);
}

__global__ __launch_bounds__(320, 2) void k_fused(const unsigned short* __restrict__ x, const unsigned short* __restrict__ w,
                                                  const unsigned short* __restrict__ gamma, const unsigned short* __restrict__ beta,
                                                  unsigned short* __restrict__ out, unsigned char* __restrict__ ws) {
  __shared__ __attribute__((aligned(16))) unsigned char smem[LDS_BYTES];
  const int g = blockIdx.x, b = g >= NT ? 1 : 0, tile = g - b * NT;
  const int wave = __builtin_amdgcn_readfirstlane(threadIdx.x >> 6), lane = threadIdx.x & 63;
  const int rot = g & 3;
  const __amdgpu_buffer_rsrc_t rsW = __builtin_amdgcn_make_buffer_rsrc((void*)ws, 0, WS_BYTES, 0x00020000);
  u64 ts[12];
  for (int i = 0; i < 12; ++i) ts[i] = 0;
  u64 tp0 = __builtin_amdgcn_s_memrealtime(); __builtin_amdgcn_sched_barrier(0);

  __builtin_amdgcn_sched_barrier(0); u64 tp1 = __builtin_amdgcn_s_memrealtime(); __builtin_amdgcn_sched_barrier(0);
  __syncthreads();
  TS(0);
  if (wave == 0) {
    const __amdgpu_buffer_rsrc_t rsX = __builtin_amdgcn_make_buffer_rsrc((void*)x, 0, 2 * T_ * D_ * 2, 0x00020000);
    const int lr = lane >> 3;
    const int v0 = lr * 1024 + (((lane & 7) ^ (lr >> 1)) << 4), v1 = v0 ^ 64;
    const int xbase = (b * T_ + tile * 128) * 512;
#pragma unroll
    for (int ci = 0; ci < 4; ++ci) {
      const int cb = xbase + (((ci + rot) & 3) << 7);
#pragma unroll
      for (int q = 0; q < 16; ++q)
        __builtin_amdgcn_raw_ptr_buffer_load_lds(rsX, (LAS void*)(smem + ci * CH + q * 1024), 16, (q & 1) ? v1 : v0,
                                                 cb + (q & 7) * 8192 + (q < 8 ? 512 : 0), 0, 2);
      if (lane < 8) __builtin_amdgcn_raw_ptr_buffer_load_lds(rsX, (LAS void*)(smem + ci * CH + 16384), 16, v0, cb + 65536, 0, 2);
    }
    TS(1);
    asm volatile("s_waitcnt vmcnt(51)" ::: "memory"); TS(2); __builtin_amdgcn_s_barrier();
    asm volatile("s_waitcnt vmcnt(34)" ::: "memory"); TS(3); __builtin_amdgcn_s_barrier();
    asm volatile("s_waitcnt vmcnt(17)" ::: "memory"); TS(4); __builtin_amdgcn_s_barrier();
    asm volatile("s_waitcnt vmcnt(0)" ::: "memory"); TS(5); __builtin_amdgcn_s_barrier();
    float gm = 0.f, bt = 0.f; u32 nonce = 0;
    if (tile < 32) {
      gm = bf2f(gamma[tile * 8 + (lane & 7)]); bt = bf2f(beta[tile * 8 + (lane & 7)]);
      nonce = *(LAS u32*)(smem + 4 * CH);
    }
    __builtin_amdgcn_s_barrier();
    TS(6);
    if (tile < 32) {
      const int off0 = WS_SLOT + (b * NT + lr) * 4096 + tile * 128 + (lane & 7) * 16;
      float s, q;
      for (u32 spins = 0;; ++spins) {
        u32x4 v[16];
        asm volatile("" ::: "memory");
#pragma unroll
        for (int i = 0; i < 16; ++i) v[i] = __builtin_amdgcn_raw_buffer_load_b128(rsW, off0 + (i == 15 && lr >= 5 ? 0 : i * 32768), 0, AUX_SC1);
        bool ok = true; s = 0.f; q = 0.f;
#pragma unroll
        for (int i = 0; i < 16; ++i) {
          ok &= (v[i].y == nonce) & (v[i].w == nonce);
          const bool use = !(i == 15 && lr >= 5);
          s += use ? __uint_as_float(v[i].x) : 0.f; q += use ? __uint_as_float(v[i].z) : 0.f;
        }
        if (__all(ok) || spins > SPIN_CAP) break;
        __builtin_amdgcn_s_sleep(1);
      }
      TS(7);
      s += __shfl_xor(s, 8); q += __shfl_xor(q, 8);
      s = add_x16(s, s); q = add_x16(q, q);
      s = add_x32(s, s); q = add_x32(q, q);
      const float mean = s * (1.0f / L_);
      float var = q * (1.0f / L_) - mean * mean; var = var < 0.f ? 0.f : var;
      const float a = gm * __builtin_amdgcn_rsqf(var + 1e-5f), c = bt - mean * a;
      u32 ua = __float_as_uint(a), uc = __float_as_uint(c);
      ua = ua == 0xFFFFFFFFu ? 0x7FC00000u : ua; uc = uc == 0xFFFFFFFFu ? 0x7FC00000u : uc;
      const int src = (lane & 3) * 2;
      u32x4 o; o.x = __shfl(ua, src); o.y = __shfl(uc, src); o.z = __shfl(ua, src + 1); o.w = __shfl(uc, src + 1);
      const int roff = WS_RES + (b * NT + (lane >> 2)) * 2048 + tile * 64 + (lane & 3) * 16;
#pragma unroll
      for (int i = 0; i < 8; ++i)
        if (i < 7 || (lane >> 2) < 13) __builtin_amdgcn_raw_buffer_store_b128(o, rsW, roff + i * 32768, 0, AUX_SC1);
      TS(8);
    }
  } else {
    const int wc = wave - 1, n = lane & 15, q4 = lane >> 4;
    const __amdgpu_buffer_rsrc_t rsB = __builtin_amdgcn_make_buffer_rsrc((void*)w, 0, 768 * 512, 0x00020000);
    if (lane < 32) __builtin_amdgcn_raw_buffer_store_b128((u32x4){~0u, ~0u, ~0u, ~0u}, rsW, WS_RES + g * 2048 + wc * 512 + lane * 16, 0, AUX_SC1);
    u32 old = 0;
    if (tile < 32) {
#pragma unroll
      for (int i = 0; i < 4; ++i) {
        const int p = (wc * 4 + i) * 8 + (lane >> 3);
        if (p < NT) __builtin_amdgcn_raw_buffer_store_b128((u32x4){0u, 0u, 0u, 0u}, rsW, WS_SLOT + (b * NT + p) * 4096 + tile * 128 + (lane & 7) * 16, 0, AUX_SC1);
      }
      old = __builtin_amdgcn_raw_buffer_load_b32(rsW, WS_NONCE + (b * 32 + tile) * 4, 0, AUX_SC1);
    }
    __builtin_amdgcn_sched_barrier(0);
    const int wv = q4 * 4096 + wc * 128 + n * 8;
    u32x2 raw[6][8];
#pragma unroll
    for (int s = 0; s < 6; ++s)
#pragma unroll
      for (int j = 0; j < 8; ++j)
        raw[s][j] = __builtin_amdgcn_raw_buffer_load_b64(rsB, wv + j * 512, ((s >> 1) * 256 + rot * 64 + (s & 1) * 32) * 512, 0);
    __builtin_amdgcn_sched_barrier(0);
    TS(1);
    const int pr = n < 4 ? 2 * n : (n < 12 ? 2 * n - 7 : 2 * n - 16);
    int aoff[2][2];
#pragma unroll
    for (int sh = 0; sh < 2; ++sh)
#pragma unroll
      for (int ks = 0; ks < 2; ++ks) { const int j = pr + sh; aoff[sh][ks] = j * 128 + (((4 * ks + q4) ^ ((j >> 1) & 7)) << 4); }
    f32x4 acc[4][4];
#pragma unroll
    for (int i = 0; i < 4; ++i)
#pragma unroll
      for (int t = 0; t < 4; ++t) acc[i][t] = (f32x4){0.f, 0.f, 0.f, 0.f};
    asm volatile("s_waitcnt vmcnt(48)" ::: "memory");
    u32 nonce_in = 0;
    for (int ci = 0; ci < 4; ++ci) {
      asm volatile("" ::: "memory"); __builtin_amdgcn_s_barrier(); asm volatile("" ::: "memory");
      if (ci == 0) TS(2); else if (ci == 1) TS(3); else if (ci == 2) TS(4); else TS(5);
      if (ci == 0 && tile < 32 && wc == 0) {
        u32 nn = old + 1u; nn = nn ? nn : 1u;
        if (lane == 0) { __builtin_amdgcn_raw_buffer_store_b32(nn, rsW, WS_NONCE + (b * 32 + tile) * 4, 0, AUX_SC1); *(LAS u32*)(smem + 4 * CH) = nn; }
      }
      if (ci == 3) nonce_in = __builtin_amdgcn_raw_buffer_load_b32(rsW, WS_NONCE + (b * 32 + wc * 8 + (n >> 1)) * 4, 0, AUX_SC1);
      const int nxt = ci < 3 ? ((ci + 1 + rot) & 3) * 64 * 512 : 0x7FFF0000 - 0x60000;
      const LAS unsigned char* cb = (const LAS unsigned char*)smem + ci * CH;
#pragma unroll
      for (int s = 0; s < 6; ++s) {
        const int tap = s >> 1, ks = s & 1;
        bf16x8 af[4];
#pragma unroll
        for (int rt = 0; rt < 4; ++rt) af[rt] = *(const LAS bf16x8*)(cb + aoff[tap == 2][ks] + rt * 2048 + (tap == 1 ? 0 : 8192));
        u32x4 bfr[4];
#pragma unroll
        for (int p = 0; p < 4; ++p) {
          bfr[0][p] = __builtin_amdgcn_perm(raw[s][2 * p + 1].x, raw[s][2 * p].x, 0x05040100u);
          bfr[1][p] = __builtin_amdgcn_perm(raw[s][2 * p + 1].x, raw[s][2 * p].x, 0x07060302u);
          bfr[2][p] = __builtin_amdgcn_perm(raw[s][2 * p + 1].y, raw[s][2 * p].y, 0x05040100u);
          bfr[3][p] = __builtin_amdgcn_perm(raw[s][2 * p + 1].y, raw[s][2 * p].y, 0x07060302u);
        }
#pragma unroll
        for (int j = 0; j < 8; ++j)
          raw[s][j] = __builtin_amdgcn_raw_buffer_load_b64(rsB, wv + j * 512, nxt + (tap * 256 + ks * 32) * 512, 0);
#pragma unroll
        for (int rt = 0; rt < 4; ++rt)
#pragma unroll
          for (int t = 0; t < 4; ++t)
            acc[rt][t] = __builtin_amdgcn_mfma_f32_16x16x32_bf16(af[rt], __builtin_bit_cast(bf16x8, bfr[t]), acc[rt][t], 0, 0, 0);
        __builtin_amdgcn_sched_barrier(0);
      }
    }
    TS(6);
    if (tile == NT - 1 && q4 == 2) {
#pragma unroll
      for (int t = 0; t < 4; ++t) acc[3][t][3] = 0.f;
    }
    float sm[4], sq[4];
#pragma unroll
    for (int t = 0; t < 4; ++t) {
      float s = 0.f, q = 0.f;
#pragma unroll
      for (int rt = 0; rt < 4; ++rt)
#pragma unroll
        for (int r = 0; r < 4; ++r) { const float v = acc[rt][t][r]; s += v; q = __builtin_fmaf(v, v, q); }
      sm[t] = s; sq[t] = q;
    }
    const float S = add_x16(add_x32(sm[0], sm[2]), add_x32(sm[1], sm[3]));
    const float Q = add_x16(add_x32(sq[0], sq[2]), add_x32(sq[1], sq[3]));
    const int soff = WS_SLOT + g * 4096 + (wc * 64 + n * 4 + q4) * 16;
    __builtin_amdgcn_raw_buffer_store_b128((u32x4){__float_as_uint(S), nonce_in, __float_as_uint(Q), nonce_in}, rsW, soff, 0, AUX_SC1);
    TS(7);
    __builtin_amdgcn_s_barrier();
    u32x4 r0, r1;
    const int roff = WS_RES + g * 2048 + wc * 512 + n * 32;
    for (u32 spins = 0;; ++spins) {
      asm volatile("" ::: "memory");
      r0 = __builtin_amdgcn_raw_buffer_load_b128(rsW, roff, 0, AUX_SC1);
      r1 = __builtin_amdgcn_raw_buffer_load_b128(rsW, roff + 16, 0, AUX_SC1);
      const bool ok = (r0.x != ~0u) & (r0.y != ~0u) & (r0.z != ~0u) & (r0.w != ~0u) & (r1.x != ~0u) & (r1.y != ~0u) & (r1.z != ~0u) & (r1.w != ~0u);
      if (__all(ok) || spins > SPIN_CAP) break;
      if ((spins & 15u) == 15u) {
        const u32 nn = __builtin_amdgcn_raw_buffer_load_b32(rsW, WS_NONCE + (b * 32 + wc * 8 + (n >> 1)) * 4, 0, AUX_SC1);
        if (nn != nonce_in) { nonce_in = nn; __builtin_amdgcn_raw_buffer_store_b128((u32x4){__float_as_uint(S), nn, __float_as_uint(Q), nn}, rsW, soff, 0, AUX_SC1); }
      }
      __builtin_amdgcn_s_sleep(1);
    }
    TS(8);
    const float a0 = __uint_as_float(r0.x), c0 = __uint_as_float(r0.y), a1 = __uint_as_float(r0.z), c1 = __uint_as_float(r0.w);
    const float a2 = __uint_as_float(r1.x), c2 = __uint_as_float(r1.y), a3 = __uint_as_float(r1.z), c3 = __uint_as_float(r1.w);
    const __amdgpu_buffer_rsrc_t rsO = __builtin_amdgcn_make_buffer_rsrc((void*)(out + (size_t)b * L_ * D_), 0, L_ * D_ * 2, 0x00020000);
    const int rb = q4 == 0 ? 0 : (q4 == 1 ? 1 : (q4 == 2 ? 9 : 8));
    const int ov = rb * 512 + wc * 128 + n * 8;
#pragma unroll
    for (int rt = 0; rt < 4; ++rt)
#pragma unroll
      for (int r = 0; r < 4; ++r) {
        u32x2 o;
        o.x = pk_bf16(gelu_tanh(__builtin_fmaf(acc[rt][0][r], a0, c0)), gelu_tanh(__builtin_fmaf(acc[rt][1][r], a1, c1)));
        o.y = pk_bf16(gelu_tanh(__builtin_fmaf(acc[rt][2][r], a2, c2)), gelu_tanh(__builtin_fmaf(acc[rt][3][r], a3, c3)));
        __builtin_amdgcn_raw_buffer_store_b64(o, rsO, ov + r * 1024, (tile * 64 + rt * 16) * 512, 16);
      }
    TS(9);
    asm volatile("s_waitcnt vmcnt(0)" ::: "memory");
    TS(10);
  }
  if (wave > 0) { const u64 tend = __builtin_amdgcn_s_memrealtime() + AMP * (tp1 - tp0); u32 sp = 0; while (__builtin_amdgcn_s_memrealtime() < tend && ++sp < 1000000) __builtin_amdgcn_s_sleep(2); }
}

extern "C" void kernel_launch(void* const* d_in, const int* in_sizes, int n_in, void* d_out, int out_size, void* d_ws, size_t ws_size, hipStream_t stream) {
  k_fused<<<NWG, 320, 0, stream>>>((const unsigned short*)d_in[0], (const unsigned short*)d_in[1], (const unsigned short*)d_in[2],
                                   (const unsigned short*)d_in[3], (unsigned short*)d_out, (unsigned char*)d_ws);
}
extern "C" void debug_report(const void* p, size_t n) {
  const u64* d = (const u64*)p;
  u64 t0 = ~0ull;
  for (int g = 0; g < NWG; ++g) for (int w = 0; w < 5; ++w) t0 = std::min(t0, d[(size_t)(g * 8 + w) * 16]);
  for (int cls = 0; cls < 2; ++cls) {
    printf(cls ? "mfma waves:\n" : "helper wave:\n");
    for (int i = 0; i < 12; ++i) {
      std::vector<double> v;
      for (int g = 0; g < NWG; ++g) for (int w = cls ? 1 : 0; w < (cls ? 5 : 1); ++w) { u64 t = d[(size_t)(g * 8 + w) * 16 + i]; if (t) v.push_back((double)(t - t0) / 100.0); }
      if (v.empty()) continue;
      std::sort(v.begin(), v.end());
      printf("  ts%-2d n=%4zu min %6.2f p10 %6.2f med %6.2f p90 %6.2f max %6.2f\n", i, v.size(), v[0], v[v.size() / 10], v[v.size() / 2], v[v.size() * 9 / 10], v.back());
    }
  }
}
```

```cpp
#include <hip/hip_runtime.h>
typedef unsigned u32;
typedef unsigned long long u64;
typedef u32 u32x2 __attribute__((ext_vector_type(2)));
typedef u32 u32x4 __attribute__((ext_vector_type(4)));
typedef float f32x2 __attribute__((ext_vector_type(2)));
typedef float f32x4 __attribute__((ext_vector_type(4)));
typedef float f32x16 __attribute__((ext_vector_type(16)));
typedef short s16x4 __attribute__((ext_vector_type(4)));
typedef short bf16x8 __attribute__((ext_vector_type(8)));
#define LAS __attribute__((address_space(3)))

constexpr int T_ = 16000, L_ = 7999, GS = 63, TR = 127;
constexpr int SLOT = 16384;
constexpr int AUX_SC1 = 16;
constexpr int WS_NONCE = 0;
constexpr int WS_RES = 4096;
constexpr int WS_PART = WS_RES + 256 * 1024;
constexpr int WS_BYTES = WS_PART + 256 * 1024;
constexpr int PF_LANES = 8;
constexpr u32 SPIN_CAP = 1u << 16;

#define SCHED 1
#define AHEAD 3
#define TS(i) do {} while (0)
#define MEMF asm volatile("" ::: "memory")
#define SBAR __builtin_amdgcn_sched_barrier(0)
#define WAITL(n) __builtin_amdgcn_s_waitcnt(0xC07F | ((n) << 8))

__device__ __forceinline__ u32 pk_bf16(f32x2 v) {
  typedef __bf16 bf16x2 __attribute__((ext_vector_type(2)));
  return __builtin_bit_cast(u32, __builtin_convertvector(v, bf16x2));
}
__device__ __forceinline__ f32x2 gelu2(f32x2 y) {
  const float c0 = -2.0f * 0.7978845608028654f * 1.4426950408889634f, c1 = c0 * 0.044715f;
  const f32x2 z = y * (y * y * c1 + c0);
  f32x2 e; e.x = __builtin_amdgcn_exp2f(z.x); e.y = __builtin_amdgcn_exp2f(z.y);
  e += 1.0f;
  f32x2 r; r.x = __builtin_amdgcn_rcpf(e.x); r.y = __builtin_amdgcn_rcpf(e.y);
  return y * r;
}
__device__ __forceinline__ float add_x32(float a, float b) {
  auto r = __builtin_amdgcn_permlane32_swap(__float_as_uint(a), __float_as_uint(b), false, false);
  return __uint_as_float(r[0]) + __uint_as_float(r[1]);
}
__device__ __forceinline__ float add_x16(float a, float b) {
  auto r = __builtin_amdgcn_permlane16_swap(__float_as_uint(a), __float_as_uint(b), false, false);
  return __uint_as_float(r[0]) + __uint_as_float(r[1]);
}
__device__ __forceinline__ float add_x8(float a) { return a + __uint_as_float(__builtin_amdgcn_update_dpp(0u, __float_as_uint(a), 0x128, 0xf, 0xf, false)); }

struct Sched { int unit[20], stage[12], slot[20], freeat[20], xpos[12], wpos[12], need[12], part; };
constexpr Sched make_sched(int cls) {
  Sched s{};
  enum { O = 0, E = 1, W0 = 2, W1 = 3, W2 = 4 };
  constexpr int XF[5] = {O, E, W1, W0, W2}, WF[5] = {W0, W2, W1, E, O}, SA[5] = {O, W1, W0, E, W2}, SB[5] = {E, W0, W2, O, W1}, SI[5] = {O, W1, E, W0, W2};
  for (int r = 0; r < 4; ++r) {
    const int* t = XF; bool ofirst = true;
    if (SCHED == 0) { const bool xf = ((r ^ cls) & 1) == 0; t = xf ? XF : WF; ofirst = xf; }
    if (SCHED == 1) { t = cls ? SB : SA; ofirst = !cls; }
    if (SCHED == 2) { if (r == 0 || r == 3) { t = cls ? SB : SA; ofirst = !cls; } else { const bool xf = ((r ^ cls) & 1) != 0; t = xf ? XF : WF; ofirst = xf; } }
    if (SCHED == 3) { t = SI; ofirst = true; }
    if (SCHED == 4) { if (r == 0) { t = cls ? SB : SA; ofirst = !cls; } else { const bool xf = ((r ^ cls) & 1) != 0; t = xf ? XF : WF; ofirst = xf; } }
    for (int p = 0; p < 5; ++p) s.unit[r * 5 + p] = t[p] | (r << 3);
    const int so[3] = {1, 0, 2}, se[3] = {0, 2, 1};
    for (int k = 0; k < 3; ++k) s.stage[r * 3 + k] = (ofirst ? so[k] : se[k]) | (r << 2);
  }
  if (SCHED == 5) {
    constexpr int a[20] = {O, W1, 8 + O, W0, E, W2,   8 + W1, 16 + O, 8 + W0, 8 + E, 8 + W2,   16 + W1, 24 + O, 16 + W0, 16 + E, 16 + W2,   24 + W1, 24 + W0, 24 + E, 24 + W2};
    constexpr int b[20] = {E, W0, 8 + E, W2, O, W1,   8 + W0, 16 + E, 8 + W2, 8 + O, 8 + W1,   16 + W0, 24 + E, 16 + W2, 16 + O, 16 + W1,   24 + W0, 24 + W2, 24 + O, 24 + W1};
    for (int u = 0; u < 20; ++u) s.unit[u] = cls ? b[u] : a[u];
    const int so[3] = {1, 0, 2}, se[3] = {0, 2, 1};
    for (int k = 0; k < 12; ++k) s.stage[k] = (cls ? se[k % 3] : so[k % 3]) | ((k / 3) << 2);
  }
  if (SCHED == 6) {
    constexpr int a[20] = {O, W1, 8 + O, W0, E, W2,   8 + W1, 8 + W0, 8 + E, 16 + O, 24 + O, 8 + W2,   16 + W1, 16 + W0, 16 + E, 16 + W2,   24 + W1, 24 + W0, 24 + E, 24 + W2};
    constexpr int b[20] = {E, W0, 8 + E, W2, O, W1,   8 + W0, 8 + W2, 8 + O, 16 + E, 24 + E, 8 + W1,   16 + W0, 16 + W2, 16 + O, 16 + W1,   24 + W0, 24 + W2, 24 + O, 24 + W1};
    for (int u = 0; u < 20; ++u) s.unit[u] = cls ? b[u] : a[u];
    const int so[3] = {1, 0, 2}, se[3] = {0, 2, 1};
    for (int k = 0; k < 12; ++k) s.stage[k] = (cls ? se[k % 3] : so[k % 3]) | ((k / 3) << 2);
  }
  int last[20] = {};
  for (int k = 0; k < 12; ++k) {
    const int tap = s.stage[k] & 3, c = s.stage[k] >> 2;
    for (int u = 0; u < 20; ++u) {
      if (s.unit[u] == ((tap == 1 ? O : E) | (c << 3))) { s.xpos[k] = u; last[u] = k; }
      if (s.unit[u] == ((2 + tap) | (c << 3))) { s.wpos[k] = u; last[u] = k; }
    }
    s.need[k] = (s.xpos[k] > s.wpos[k] ? s.xpos[k] : s.wpos[k]) + 1;
  }
  int avail[10] = {};
  for (int u = 0; u < 20; ++u) {
    int best = 0;
    for (int q = 1; q < 10; ++q) if (avail[q] < avail[best]) best = q;
    s.slot[u] = best; s.freeat[u] = avail[best]; avail[best] = last[u] + 1;
  }
  s.part = 0;
  for (int q = 0; q < 10; ++q) if ((q < 5 || q == 9) && avail[q] < avail[s.part]) s.part = q;
  return s;
}
constexpr Sched S0 = make_sched(0), S1 = make_sched(1);
constexpr u64 tab(const Sched& s, int what) {
  u64 t = 0;
  for (int k = 0; k < 12; ++k) t |= (u64)(what == 0 ? (s.slot[s.xpos[k]] | (((s.stage[k] & 3) == 2) << 4)) : s.slot[s.wpos[k]]) << (5 * k);
  return t;
}
constexpr u64 TX0 = tab(S0, 0), TX1 = tab(S1, 0), TW0 = tab(S0, 1), TW1 = tab(S1, 1);
constexpr int PART0 = S0.part, PART1 = S1.part;
constexpr int need_of(int j) { int n = 0; for (int k = 0; k <= j; ++k) { n = S0.need[k] > n ? S0.need[k] : n; n = S1.need[k] > n ? S1.need[k] : n; } return n; }
constexpr int issued_before(int j) {
  if (j < 0) return 0;
  int n = issued_before(j - 1);
  while (n < 20 && n < need_of(j) + AHEAD && (S0.freeat[n] == 0 || S0.freeat[n] < j) && (S1.freeat[n] == 0 || S1.freeat[n] < j)) ++n;
  return n;
}
constexpr bool sched_ok() { for (int j = 0; j < 12; ++j) if (issued_before(j) < need_of(j)) return false; return issued_before(11) == 20; }
static_assert(sched_ok(), "schedule deadlocks");
template <int N> struct IC { static constexpr int v = N; };
template <int... I> struct Seq {};
template <int N, int... I> struct MkSeq : MkSeq<N - 1, N - 1, I...> {};
template <int... I> struct MkSeq<0, I...> { typedef Seq<I...> t; };
template <class F, int... I> __device__ __forceinline__ void static_for(F&& f, Seq<I...>) { (f(IC<I>{}), ...); }
constexpr int L_H = 5 * SLOT;

__global__ __launch_bounds__(512, 2) void k_fused(const unsigned short* __restrict__ x, const unsigned short* __restrict__ w,
                                                  unsigned char* __restrict__ ws, unsigned short* __restrict__ out,
                                                  const unsigned short* __restrict__ gamma, const unsigned short* __restrict__ beta) {
  __shared__ __attribute__((aligned(16))) unsigned char smem[10 * SLOT];
  u64 pc0; asm volatile("s_getpc_b64 %0" : "=s"(pc0));
  const int g = blockIdx.x;
  const int rt = (g >> 4) * 8 + (g & 7), half = (g >> 3) & 1;
  if (rt >= 2 * GS) return;
  const int b = rt >= GS ? 1 : 0, m = rt - b * GS, grp = b * 2 + half;
  const int rot = g >> 4, cls = half ^ ((g >> 6) & 1);
  const int tid = threadIdx.x, wave = __builtin_amdgcn_readfirstlane(tid >> 6), lane = tid & 63;
  const u32 lds0 = (u32)(size_t)(LAS unsigned char*)smem;
  const int lpart = (cls ? PART1 : PART0) * SLOT;
  const __amdgpu_buffer_rsrc_t rsS = __builtin_amdgcn_make_buffer_rsrc((void*)ws, 0, WS_BYTES, 0x00020000);
  TS(0);
  f32x4 hv[4][2];
  if (__builtin_expect(wave < 4, 1)) {
    u32x4 dX, dW, dP, dQ;
    dX.x = (u32)(size_t)x; dX.y = (u32)((size_t)x >> 32) & 0xffffu; dX.z = 2 * T_ * 512; dX.w = 0x00020000u;
    dW.x = (u32)((size_t)w - 1024); dW.y = (u32)(((size_t)w - 1024) >> 32) & 0xffffu; dW.z = 768 * 512 + 1024; dW.w = 0x00020000u;
    dP = cls ? dW : dX; dQ = cls ? dX : dW;
    int vX[4], vW[4], vP[4], vQ[4];
    const int r8 = (lane >> 2) & 7, c16 = ((lane >> 5) << 2) | ((lane & 3) ^ (r8 >> 2));
#pragma unroll
    for (int i = 0; i < 4; ++i) {
      vX[i] = (wave * 32 + i * 7 + r8) * 1024 + ((c16 ^ ((i & 1) << 1)) << 4);
      vW[i] = (wave * 16 + (i >> 1) * 8 + r8) * 512 + half * 256 + (i & 1) * 128 + ((c16 ^ (i & 2)) << 4) + (1 - i) * 1024;
      vP[i] = cls ? vW[i] : vX[i]; vQ[i] = cls ? vX[i] : vW[i];
    }
    const int xbase = (b * T_ + m * (2 * TR)) * 512;
    const u32 mbase = lds0 + wave * 4096;
    const bool red = m < 8;
    const int lr = lane >> 3;
    u32 old = 0;
    static_for([&](auto J) {
      constexpr int j = J.v, lo = issued_before(j - 1), hi = issued_before(j);
      static_for([&](auto U) {
        constexpr int u = lo + U.v;
        constexpr int k0 = S0.unit[u] & 7, k1 = S1.unit[u] & 7, r0 = S0.unit[u] >> 3, r1 = S1.unit[u] >> 3, sl0 = S0.slot[u], sl1 = S1.slot[u];
        const int c0 = (r0 + rot) & 3, c1 = (r1 + rot) & 3;
        const int so0 = k0 < 2 ? xbase + c0 * 128 + (k0 == 0 ? 512 : 0) : c0 * 32768 + (k0 - 2) * 131072;
        const int so1 = k1 < 2 ? xbase + c1 * 128 + (k1 == 0 ? 512 : 0) : c1 * 32768 + (k1 - 2) * 131072;
        const int so = cls ? so1 : so0;
        const u32 m0v = mbase + (cls ? sl1 : sl0) * SLOT;
        const u32x4 d = k0 < 2 ? (k1 < 2 ? dX : dP) : (k1 < 2 ? dQ : dW);
        const int* v = k0 < 2 ? (k1 < 2 ? vX : vP) : (k1 < 2 ? vQ : vW);
        asm volatile("s_mov_b32 m0, %0\n\ts_nop 0\n\t"
                     "buffer_load_dwordx4 %1, %5, %6 offen lds\n\tbuffer_load_dwordx4 %2, %5, %6 offen offset:1024 lds\n\t"
                     "buffer_load_dwordx4 %3, %5, %6 offen offset:2048 lds\n\tbuffer_load_dwordx4 %4, %5, %6 offen offset:3072 lds"
                     :: "s"(m0v), "v"(v[0]), "v"(v[1]), "v"(v[2]), "v"(v[3]), "s"(d), "s"(so) : "memory");
      }, typename MkSeq<hi - lo>::t{});
      asm volatile("s_waitcnt vmcnt(%0)\n\ts_barrier" :: "i"(4 * (hi - need_of(j))) : "memory");
      if (j == 1) {
        if (wave == 2) __builtin_amdgcn_raw_buffer_store_b128((u32x4){~0u, ~0u, ~0u, ~0u}, rsS, WS_RES + (grp * 64 + m) * 1024 + lane * 16, 0, AUX_SC1);
        if (wave == 1 && red) {
#pragma unroll
          for (int i = 0; i < 8; ++i) __builtin_amdgcn_raw_buffer_store_b128((u32x4){0u, 0u, 0u, 0u}, rsS, WS_PART + (grp * 64 + lr) * 1024 + m * 128 + (lane & 7) * 16, i * 8192, AUX_SC1);
          asm volatile("buffer_load_dword %0, %1, %2, 0 offen sc1" : "=v"(old) : "v"(WS_NONCE + (grp * 8 + m) * 4), "s"(rsS) : "memory");
        }
      }
      if (j == 5) {
        static_assert(need_of(5) > issued_before(1), "");
        if (wave == 1 && red) {
          asm volatile("" : "+v"(old));
          old = (old & 3u) == 1u ? 2u : 1u;
          if (lane == 0) __builtin_amdgcn_raw_buffer_store_b32(old, rsS, WS_NONCE + (grp * 8 + m) * 4, 0, AUX_SC1);
        }
      }
    }, MkSeq<12>::t{});
    TS(5);
    u32 nin = old; float gm0 = 0.f, gm1 = 0.f, bt0 = 0.f, bt1 = 0.f;
    const int noff = WS_NONCE + (grp * 8 + lr) * 4;
    if (wave == 0) nin = __builtin_amdgcn_raw_buffer_load_b32(rsS, noff, 0, AUX_SC1);
    if (wave == 1 && red) {
      const int ch = half * 128 + m * 16 + (lane & 7) * 2;
      const u32 gg = *(const u32*)(gamma + ch), bb = *(const u32*)(beta + ch);
      gm0 = __uint_as_float(gg << 16); gm1 = __uint_as_float(gg & 0xffff0000u); bt0 = __uint_as_float(bb << 16); bt1 = __uint_as_float(bb & 0xffff0000u);
    }
    MEMF; __builtin_amdgcn_s_barrier(); MEMF;
    TS(6);
    u32x4 pub;
    const int poff = WS_PART + (grp * 64 + m) * 1024 + lane * 16;
    if (wave == 0) {
      const f32x4 p0 = *(const LAS f32x4*)(smem + lpart + lane * 16), p1 = *(const LAS f32x4*)(smem + lpart + 1024 + lane * 16);
      pub = __builtin_bit_cast(u32x4, p0 + p1) & ~3u;
      u32 t = nin & 3u; t = t == 3u ? 0u : t;
      __builtin_amdgcn_raw_buffer_store_b128(pub | t, rsS, poff, 0, AUX_SC1);
      TS(7);
    }
    if (wave == 1 && red) {
      const int off0 = WS_PART + (grp * 64 + lr) * 1024 + m * 128 + (lane & 7) * 16;
      u32x4 v[8];
      _Pragma("unroll 1") for (u32 sp = 0;; ++sp) {
        MEMF;
#pragma unroll
        for (int i = 0; i < 8; ++i) v[i] = __builtin_amdgcn_raw_buffer_load_b128(rsS, off0 + (i == 7 && lr == 7 ? 0 : i * 8192), 0, AUX_SC1);
        u32x4 a = v[0];
#pragma unroll
        for (int i = 1; i < 8; ++i) a &= v[i];
        if (__all((a.x & a.y & a.z & a.w & nin) != 0u) || sp > SPIN_CAP) break;
      }
      TS(7);
      f32x4 s = lr == 7 ? (f32x4){0.f, 0.f, 0.f, 0.f} : __builtin_bit_cast(f32x4, v[7] & ~3u);
#pragma unroll
      for (int i = 0; i < 7; ++i) s += __builtin_bit_cast(f32x4, v[i] & ~3u);
#pragma unroll
      for (int e = 0; e < 4; ++e) { float t = add_x8(s[e]); t = add_x16(t, t); s[e] = add_x32(t, t); }
      const float mu0 = s.x * (1.0f / L_), mu1 = s.z * (1.0f / L_);
      const float v0 = fmaxf(s.y * (1.0f / L_) - mu0 * mu0, 0.f), v1 = fmaxf(s.w * (1.0f / L_) - mu1 * mu1, 0.f);
      const float a0 = gm0 * __builtin_amdgcn_rsqf(v0 + 1e-5f), a1 = gm1 * __builtin_amdgcn_rsqf(v1 + 1e-5f);
      u32x4 o = {__float_as_uint(a0), __float_as_uint(bt0 - mu0 * a0), __float_as_uint(a1), __float_as_uint(bt1 - mu1 * a1)};
#pragma unroll
      for (int e = 0; e < 4; ++e) o[e] = o[e] == 0xFFFFFFFFu ? 0x7FC00000u : o[e];
      const int roff = WS_RES + (grp * 64 + lr) * 1024 + m * 128 + (lane & 7) * 16;
#pragma unroll
      for (int i = 0; i < 8; ++i) __builtin_amdgcn_raw_buffer_store_b128(o, rsS, roff + i * 8192, 0, AUX_SC1);
      TS(8);
    }
    _Pragma("unroll 1") for (u32 sp = 0; sp < SPIN_CAP; ++sp) {
      const u32x4 d = *(volatile const LAS u32x4*)(smem + lpart + 4096);
      if (__builtin_amdgcn_readfirstlane(d.x & d.y & d.z & d.w)) break;
      __builtin_amdgcn_s_sleep(2);
    }
    MEMF;
#pragma unroll
    for (int i = 0; i < 4; ++i)
#pragma unroll
      for (int e = 0; e < 2; ++e) hv[i][e] = *(const LAS f32x4*)(smem + L_H + ((tid >> 4) + 32 * i) * 512 + e * 256 + (tid & 15) * 16);
    if (wave == 0) {
      u32x4 r;
      _Pragma("unroll 1") for (u32 sp = 0;; ++sp) {
        MEMF;
        r = __builtin_amdgcn_raw_buffer_load_b128(rsS, WS_RES + (grp * 64 + m) * 1024 + lane * 16, 0, AUX_SC1);
        if (__all((r.x != ~0u) & (r.y != ~0u) & (r.z != ~0u) & (r.w != ~0u)) || sp > SPIN_CAP) break;
        if ((sp & 15u) == 15u) {
          const u32 nn = __builtin_amdgcn_raw_buffer_load_b32(rsS, noff, 0, AUX_SC1);
          if (nn != nin) { nin = nn; u32 t = nn & 3u; t = t == 3u ? 0u : t; __builtin_amdgcn_raw_buffer_store_b128(pub | t, rsS, poff, 0, AUX_SC1); }
        }
      }
      *(LAS u32x4*)(smem + lpart + 2048 + lane * 16) = r;
      TS(9);
    }
  } else {
    const int wq = wave - 4, wr = wq >> 1, wc = wq & 1;
    u32 pf = 0;
    if (wq == 1 && lane < PF_LANES)
      asm volatile("global_load_dword %0, %1, %2" : "=v"(pf) : "v"(((g >> 3) & 15) * (PF_LANES * 64) + lane * 64), "s"(pc0 & ~255ull));
    const int r = lane & 31, h = lane >> 5, tq = (lane & 15) >> 2, tp = lane & 3, blk = (lane >> 4) & 1;
    const int j0 = wr * 64 + r, j1 = j0 + 1;
    const u32 ab0 = lds0 + (j0 >> 3) * 1024 + (j0 & 7) * 64 + ((h ^ ((j0 >> 2) & 3)) << 4), ab1 = lds0 + (j1 >> 3) * 1024 + (j1 & 7) * 64 + ((h ^ ((j1 >> 2) & 3)) << 4);
    const u32 wb = lds0 + h * 2048 + wc * 1024 + tq * 64 + (((2 * blk + (tp >> 1)) ^ (2 * h)) << 4) + 8 * (tp & 1);
    u64 tX = cls ? TX1 : TX0, tW = cls ? TW1 : TW0;
    f32x16 acc[2][2];
#pragma unroll
    for (int i = 0; i < 2; ++i)
#pragma unroll
      for (int j = 0; j < 2; ++j) {
#pragma unroll
        for (int e = 0; e < 16; ++e) acc[i][j][e] = 0.f;
        asm volatile("" : "+v"(acc[i][j]));
      }
    bf16x8 fa[4][2]; s16x4 fl[4][2], fh[4][2];
    u32 a0, a1, w0, w1;
#define SETA do { a0 = (((u32)tX & 16u) ? ab1 : ab0) + ((u32)tX & 15u) * SLOT; a1 = a0 ^ 32; tX >>= 5; asm volatile("" : "+v"(a0), "+v"(a1)); } while (0)
#define SETW do { w0 = wb + ((u32)tW & 15u) * SLOT; w1 = w0 ^ 16; tW >>= 5; asm volatile("" : "+v"(w0), "+v"(w1)); } while (0)
#define RDA(ks, i) fa[ks][i] = *(const LAS bf16x8*)(size_t)((((ks) & 1) ? a1 : a0) + ((ks) >> 1) * 512 + (i) * 4096)
#define RDB(ks, j) do { fl[ks][j] = __builtin_amdgcn_ds_read_tr16_b64_v4i16((LAS s16x4*)(size_t)(w0 + (j) * 512 + (ks) * 4096)); \
                        fh[ks][j] = __builtin_amdgcn_ds_read_tr16_b64_v4i16((LAS s16x4*)(size_t)(w1 + (j) * 512 + (ks) * 4096 + 256)); } while (0)
#define MM(ks, i, j) acc[i][j] = __builtin_amdgcn_mfma_f32_32x32x16_bf16(fa[ks][i], __builtin_shufflevector(fl[ks][j], fh[ks][j], 0, 1, 2, 3, 4, 5, 6, 7), acc[i][j], 0, 0, 0); SBAR
#define GRP(c, X0, X1, X2, X3) do { MM(c, 0, 0); X0; SBAR; MM(c, 0, 1); X1; SBAR; MM(c, 1, 0); X2; SBAR; MM(c, 1, 1); X3; SBAR; } while (0)
#define RD3(ks, i) RDA(ks, i); RDB(ks, i)
    SETA; SETW;
    TS(1);
    __builtin_amdgcn_s_barrier(); MEMF;
    TS(2);
    RD3(0, 0); SBAR; RD3(0, 1); SBAR; RD3(1, 0); SBAR; RD3(1, 1); SBAR;
#pragma unroll 1
    for (int k = 0; k < 11; ++k) {
      GRP(0, RD3(2, 0), RD3(2, 1), , );
      GRP(1, RD3(3, 0), RD3(3, 1), , );
      GRP(2, SETA, SETW, , WAITL(0));
      __builtin_amdgcn_s_barrier(); MEMF; SBAR;
      GRP(3, RD3(0, 0), RD3(0, 1), RD3(1, 0), RD3(1, 1));
    }
    GRP(0, RD3(2, 0), RD3(2, 1), , );
    GRP(1, RD3(3, 0), RD3(3, 1), , );
    GRP(2, , , , ); GRP(3, , , , );
    TS(6);
    {
      const int nvalid = (m == GS - 1 ? L_ - (GS - 1) * TR : TR) - wr * 64 - 32 - 4 * h;
#pragma unroll
      for (int e = 13; e < 16; ++e)
#pragma unroll
        for (int j = 0; j < 2; ++j) acc[1][j][e] = (24 + (e & 3)) < nvalid ? acc[1][j][e] : 0.f;
    }
    float sm[2], sq[2];
#pragma unroll
    for (int j = 0; j < 2; ++j) {
      f32x2 s = {0.f, 0.f}, q = {0.f, 0.f};
#pragma unroll
      for (int i = 0; i < 2; ++i)
#pragma unroll
        for (int e = 0; e < 16; e += 2) { const f32x2 v = {acc[i][j][e], acc[i][j][e + 1]}; s += v; q = __builtin_elementwise_fma(v, v, q); }
      sm[j] = s.x + s.y; sq[j] = q.x + q.y;
    }
    *(LAS f32x2*)(smem + lpart + wr * 1024 + (wc * 64 + lane) * 8) = (f32x2){add_x32(sm[0], sm[1]), add_x32(sq[0], sq[1])};
    *(volatile LAS u32*)(smem + lpart + 4096 + wq * 4) = 0u;
    asm volatile("s_waitcnt lgkmcnt(0)" ::: "memory");
    __builtin_amdgcn_s_barrier(); MEMF;
    TS(7);
    {
      const int hb = L_H + (wr * 64 + 4 * h) * 512 + (wc * 8 + (r >> 3)) * 16 + ((r >> 2) & 1) * 256 + (r & 3) * 4;
#pragma unroll
      for (int i = 0; i < 2; ++i)
#pragma unroll
        for (int j = 0; j < 2; ++j)
#pragma unroll
          for (int e = 0; e < 16; ++e) *(LAS float*)(smem + hb + (i * 32 + (e & 3) + 8 * (e >> 2)) * 512 + j * 64) = acc[i][j][e];
    }
    asm volatile("s_waitcnt vmcnt(0) lgkmcnt(0)" :: "v"(pf) : "memory");
    *(volatile LAS u32*)(smem + lpart + 4096 + wq * 4) = 1u;
    _Pragma("unroll 1") for (u32 sp = 0; sp < SPIN_CAP; ++sp) {
      const u32x4 d = *(volatile const LAS u32x4*)(smem + lpart + 4096);
      if (__builtin_amdgcn_readfirstlane(d.x & d.y & d.z & d.w)) break;
      __builtin_amdgcn_s_sleep(1);
    }
    MEMF;
#pragma unroll
    for (int i = 0; i < 4; ++i)
#pragma unroll
      for (int e = 0; e < 2; ++e) hv[i][e] = *(const LAS f32x4*)(smem + L_H + ((tid >> 4) + 32 * i) * 512 + e * 256 + (tid & 15) * 16);
    TS(8);
  }
  asm volatile("s_waitcnt lgkmcnt(0)" ::: "memory");
  __builtin_amdgcn_s_barrier(); MEMF;
  TS(10);
  {
    f32x4 ac[4];
#pragma unroll
    for (int e = 0; e < 4; ++e) ac[e] = *(const LAS f32x4*)(smem + lpart + 2048 + (tid & 15) * 64 + e * 16);
    const __amdgpu_buffer_rsrc_t rsO = __builtin_amdgcn_make_buffer_rsrc((void*)(out + (size_t)b * L_ * 256), 0, L_ * 512, 0x00020000);
    const int ov = (tid >> 4) * 512 + half * 256 + (tid & 15) * 16;
#pragma unroll
    for (int i = 0; i < 4; ++i) {
      u32x4 o;
#pragma unroll
      for (int e = 0; e < 4; ++e) {
        const f32x2 v = {hv[i][e >> 1][(e & 1) * 2], hv[i][e >> 1][(e & 1) * 2 + 1]};
        const f32x2 a = {ac[e].x, ac[e].z}, c = {ac[e].y, ac[e].w};
        o[e] = pk_bf16(gelu2(__builtin_elementwise_fma(v, a, c)));
      }
      if (i < 3 || tid < 496) __builtin_amdgcn_raw_buffer_store_b128(o, rsO, ov, (m * TR + 32 * i) * 512, AUX_SC1);
    }
  }
  TS(11);
}

extern "C" void kernel_launch(void* const* d_in, const int* in_sizes, int n_in, void* d_out, int out_size, void* d_ws, size_t ws_size, hipStream_t stream) {
  k_fused<<<256, 512, 0, stream>>>((const unsigned short*)d_in[0], (const unsigned short*)d_in[1], (unsigned char*)d_ws, (unsigned short*)d_out,
                                   (const unsigned short*)d_in[2], (const unsigned short*)d_in[3]);
}
```

```cpp
#include <hip/hip_runtime.h>
typedef unsigned u32;
typedef unsigned long long u64;
typedef u32 u32x2 __attribute__((ext_vector_type(2)));
typedef u32 u32x4 __attribute__((ext_vector_type(4)));
typedef float f32x2 __attribute__((ext_vector_type(2)));
typedef float f32x4 __attribute__((ext_vector_type(4)));
typedef float f32x16 __attribute__((ext_vector_type(16)));
typedef u32 u32x8 __attribute__((ext_vector_type(8)));
#define LAS __attribute__((address_space(3)))

#define AUXO 19
#define GDELAY 19
#define PFS 27

constexpr int T_ = 16000, L_ = 7999, GS = 63, TR = 127;
constexpr int SLOT = 16384;
constexpr int AUX_SC1 = 16;
constexpr int WS_PART = 0;
constexpr int WS_ARR = 262144;
constexpr int WS_GO = WS_ARR + 128;
constexpr int WS_NONCE = WS_ARR + 63 * 256;
constexpr int WS_BYTES = WS_ARR + 65536;
constexpr int PF_SLICES = PFS;
constexpr u32 SPIN_CAP = 1u << 16;

#define MEMF asm volatile("" ::: "memory")
#define SBAR __builtin_amdgcn_sched_barrier(0)

__device__ __forceinline__ u32 pk_bf16(f32x2 v) {
  typedef __bf16 bf16x2 __attribute__((ext_vector_type(2)));
  return __builtin_bit_cast(u32, __builtin_convertvector(v, bf16x2));
}
__device__ __forceinline__ f32x2 gelu2(f32x2 y) {
  const float c0 = -2.0f * 0.7978845608028654f * 1.4426950408889634f, c1 = c0 * 0.044715f;
  const f32x2 z = y * (y * y * c1 + c0);
  f32x2 e; e.x = __builtin_amdgcn_exp2f(z.x); e.y = __builtin_amdgcn_exp2f(z.y);
  e += 1.0f;
  f32x2 r; r.x = __builtin_amdgcn_rcpf(e.x); r.y = __builtin_amdgcn_rcpf(e.y);
  return y * r;
}
__device__ __forceinline__ float add_x32(float a, float b) {
  auto r = __builtin_amdgcn_permlane32_swap(__float_as_uint(a), __float_as_uint(b), false, false);
  return __uint_as_float(r[0]) + __uint_as_float(r[1]);
}
__device__ __forceinline__ float add_x16(float a, float b) {
  auto r = __builtin_amdgcn_permlane16_swap(__float_as_uint(a), __float_as_uint(b), false, false);
  return __uint_as_float(r[0]) + __uint_as_float(r[1]);
}
template <int N> __device__ __forceinline__ float add_ror(float a) { return a + __uint_as_float(__builtin_amdgcn_update_dpp(0u, __float_as_uint(a), 0x120 + N, 0xf, 0xf, false)); }

#define NXS 4
#define CMAP 1
#define LPB 0
#define STREAM \
  "s_add_u32 m0, s46, 0x14000\n\t" \
  "s_add_u32 %[so], s54, s48\n\t" \
  "buffer_load_dwordx4 v12, %[dw], %[so] offen lds\n\t" \
  "buffer_load_dwordx4 v13, %[dw], %[so] offen offset:1024 lds\n\t" \
  "buffer_load_dwordx4 v14, %[dw], %[so] offen offset:2048 lds\n\t" \
  "buffer_load_dwordx4 v15, %[dw], %[so] offen offset:3072 lds\n\t" \
  "s_add_u32 m0, s46, 0x4000\n\t" \
  "s_add_u32 %[so], s44, s41\n\t" \
  "buffer_load_dwordx4 v8, %[dx], %[so] offen lds\n\t" \
  "buffer_load_dwordx4 v9, %[dx], %[so] offen offset:1024 lds\n\t" \
  "buffer_load_dwordx4 v10, %[dx], %[so] offen offset:2048 lds\n\t" \
  "buffer_load_dwordx4 v11, %[dx], %[so] offen offset:3072 lds\n\t" \
  "s_add_u32 m0, s46, 0x18000\n\t" \
  "s_add_u32 %[so], s52, s49\n\t" \
  "buffer_load_dwordx4 v12, %[dw], %[so] offen lds\n\t" \
  "buffer_load_dwordx4 v13, %[dw], %[so] offen offset:1024 lds\n\t" \
  "s_waitcnt vmcnt(10)\n\t" \
  "s_barrier\n\t" \
  "buffer_load_dwordx4 v14, %[dw], %[so] offen offset:2048 lds\n\t" \
  "buffer_load_dwordx4 v15, %[dw], %[so] offen offset:3072 lds\n\t" \
  "s_add_u32 m0, s46, 0x1c000\n\t" \
  "s_add_u32 %[so], s54, s49\n\t" \
  "buffer_load_dwordx4 v12, %[dw], %[so] offen lds\n\t" \
  "buffer_load_dwordx4 v13, %[dw], %[so] offen offset:1024 lds\n\t" \
  "buffer_load_dwordx4 v14, %[dw], %[so] offen offset:2048 lds\n\t" \
  "buffer_load_dwordx4 v15, %[dw], %[so] offen offset:3072 lds\n\t" \
  "s_add_u32 m0, s46, 0x8000\n\t" \
  "s_add_u32 %[so], s45, s40\n\t" \
  "buffer_load_dwordx4 v8, %[dx], %[so] offen lds\n\t" \
  "s_waitcnt vmcnt(13)\n\t" \
  "s_barrier\n\t" \
  "buffer_load_dwordx4 v9, %[dx], %[so] offen offset:1024 lds\n\t" \
  "buffer_load_dwordx4 v10, %[dx], %[so] offen offset:2048 lds\n\t" \
  "buffer_load_dwordx4 v11, %[dx], %[so] offen offset:3072 lds\n\t" \
  "s_add_u32 m0, s46, 0x20000\n\t" \
  "s_add_u32 %[so], s53, s48\n\t" \
  "buffer_load_dwordx4 v12, %[dw], %[so] offen lds\n\t" \
  "buffer_load_dwordx4 v13, %[dw], %[so] offen offset:1024 lds\n\t" \
  "buffer_load_dwordx4 v14, %[dw], %[so] offen offset:2048 lds\n\t" \
  "buffer_load_dwordx4 v15, %[dw], %[so] offen offset:3072 lds\n\t" \
  "s_waitcnt vmcnt(12)\n\t" \
  "s_barrier\n\t" \
  "s_bitcmp1_b32 s55, 1\n\t" \
  "s_cbranch_scc0 211f\n\t" \
  "s_mov_b64 exec, 1\n\t" \
  "global_load_dword %[nin], %[vn], %[ws] sc1\n\t" \
  "s_mov_b64 exec, -1\n\t" \
  "211:\n\t" \
  "s_add_u32 m0, s46, 0xc000\n\t" \
  "s_add_u32 %[so], s45, s41\n\t" \
  "buffer_load_dwordx4 v8, %[dx], %[so] offen lds\n\t" \
  "buffer_load_dwordx4 v9, %[dx], %[so] offen offset:1024 lds\n\t" \
  "buffer_load_dwordx4 v10, %[dx], %[so] offen offset:2048 lds\n\t" \
  "buffer_load_dwordx4 v11, %[dx], %[so] offen offset:3072 lds\n\t" \
  "s_add_u32 m0, s46, 0x24000\n\t" \
  "s_add_u32 %[so], s53, s49\n\t" \
  "buffer_load_dwordx4 v12, %[dw], %[so] offen lds\n\t" \
  "s_waitcnt vmcnt(13)\n\t" \
  "s_barrier\n\t" \
  "buffer_load_dwordx4 v13, %[dw], %[so] offen offset:1024 lds\n\t" \
  "buffer_load_dwordx4 v14, %[dw], %[so] offen offset:2048 lds\n\t" \
  "buffer_load_dwordx4 v15, %[dw], %[so] offen offset:3072 lds\n\t" \
  "s_add_u32 m0, s46, 0x0\n\t" \
  "s_add_u32 %[so], s44, s42\n\t" \
  "buffer_load_dwordx4 v8, %[dx], %[so] offen lds\n\t" \
  "buffer_load_dwordx4 v9, %[dx], %[so] offen offset:1024 lds\n\t" \
  "buffer_load_dwordx4 v10, %[dx], %[so] offen offset:2048 lds\n\t" \
  "buffer_load_dwordx4 v11, %[dx], %[so] offen offset:3072 lds\n\t" \
  "s_add_u32 m0, s46, 0x10000\n\t" \
  "s_add_u32 %[so], s52, s50\n\t" \
  "buffer_load_dwordx4 v12, %[dw], %[so] offen lds\n\t" \
  "s_waitcnt vmcnt(13)\n\t" \
  "s_barrier\n\t" \
  "s_bitcmp1_b32 s55, 1\n\t" \
  "s_cbranch_scc0 212f\n\t" \
  "s_mov_b64 exec, 1\n\t" \
  "global_store_dword %[va], %[nin], %[ws] sc1\n\t" \
  "s_mov_b64 exec, -1\n\t" \
  "212:\n\t" \
  "buffer_load_dwordx4 v13, %[dw], %[so] offen offset:1024 lds\n\t" \
  "buffer_load_dwordx4 v14, %[dw], %[so] offen offset:2048 lds\n\t" \
  "buffer_load_dwordx4 v15, %[dw], %[so] offen offset:3072 lds\n\t" \
  "s_add_u32 m0, s46, 0x14000\n\t" \
  "s_add_u32 %[so], s54, s50\n\t" \
  "buffer_load_dwordx4 v12, %[dw], %[so] offen lds\n\t" \
  "buffer_load_dwordx4 v13, %[dw], %[so] offen offset:1024 lds\n\t" \
  "buffer_load_dwordx4 v14, %[dw], %[so] offen offset:2048 lds\n\t" \
  "buffer_load_dwordx4 v15, %[dw], %[so] offen offset:3072 lds\n\t" \
  "s_waitcnt vmcnt(12)\n\t" \
  "s_barrier\n\t" \
  "s_add_u32 m0, s46, 0x4000\n\t" \
  "s_add_u32 %[so], s44, s43\n\t" \
  "buffer_load_dwordx4 v8, %[dx], %[so] offen lds\n\t" \
  "buffer_load_dwordx4 v9, %[dx], %[so] offen offset:1024 lds\n\t" \
  "buffer_load_dwordx4 v10, %[dx], %[so] offen offset:2048 lds\n\t" \
  "buffer_load_dwordx4 v11, %[dx], %[so] offen offset:3072 lds\n\t" \
  "s_add_u32 m0, s46, 0x18000\n\t" \
  "s_add_u32 %[so], s52, s51\n\t" \
  "buffer_load_dwordx4 v12, %[dw], %[so] offen lds\n\t" \
  "buffer_load_dwordx4 v13, %[dw], %[so] offen offset:1024 lds\n\t" \
  "buffer_load_dwordx4 v14, %[dw], %[so] offen offset:2048 lds\n\t" \
  "s_waitcnt vmcnt(11)\n\t" \
  "s_barrier\n\t" \
  "buffer_load_dwordx4 v15, %[dw], %[so] offen offset:3072 lds\n\t" \
  "s_add_u32 m0, s46, 0x1c000\n\t" \
  "s_add_u32 %[so], s54, s51\n\t" \
  "buffer_load_dwordx4 v12, %[dw], %[so] offen lds\n\t" \
  "buffer_load_dwordx4 v13, %[dw], %[so] offen offset:1024 lds\n\t" \
  "buffer_load_dwordx4 v14, %[dw], %[so] offen offset:2048 lds\n\t" \
  "buffer_load_dwordx4 v15, %[dw], %[so] offen offset:3072 lds\n\t" \
  "s_add_u32 m0, s46, 0x8000\n\t" \
  "s_add_u32 %[so], s45, s42\n\t" \
  "buffer_load_dwordx4 v8, %[dx], %[so] offen lds\n\t" \
  "s_waitcnt vmcnt(13)\n\t" \
  "s_barrier\n\t" \
  "buffer_load_dwordx4 v9, %[dx], %[so] offen offset:1024 lds\n\t" \
  "buffer_load_dwordx4 v10, %[dx], %[so] offen offset:2048 lds\n\t" \
  "buffer_load_dwordx4 v11, %[dx], %[so] offen offset:3072 lds\n\t" \
  "s_add_u32 m0, s46, 0x20000\n\t" \
  "s_add_u32 %[so], s53, s50\n\t" \
  "buffer_load_dwordx4 v12, %[dw], %[so] offen lds\n\t" \
  "buffer_load_dwordx4 v13, %[dw], %[so] offen offset:1024 lds\n\t" \
  "buffer_load_dwordx4 v14, %[dw], %[so] offen offset:2048 lds\n\t" \
  "buffer_load_dwordx4 v15, %[dw], %[so] offen offset:3072 lds\n\t" \
  "s_waitcnt vmcnt(12)\n\t" \
  "s_barrier\n\t" \
  "s_add_u32 m0, s46, 0xc000\n\t" \
  "s_add_u32 %[so], s45, s43\n\t" \
  "buffer_load_dwordx4 v8, %[dx], %[so] offen lds\n\t" \
  "buffer_load_dwordx4 v9, %[dx], %[so] offen offset:1024 lds\n\t" \
  "buffer_load_dwordx4 v10, %[dx], %[so] offen offset:2048 lds\n\t" \
  "buffer_load_dwordx4 v11, %[dx], %[so] offen offset:3072 lds\n\t" \
  "s_add_u32 m0, s46, 0x24000\n\t" \
  "s_add_u32 %[so], s53, s51\n\t" \
  "buffer_load_dwordx4 v12, %[dw], %[so] offen lds\n\t" \
  "s_waitcnt vmcnt(13)\n\t" \
  "s_barrier\n\t" \
  "buffer_load_dwordx4 v13, %[dw], %[so] offen offset:1024 lds\n\t" \
  "buffer_load_dwordx4 v14, %[dw], %[so] offen offset:2048 lds\n\t" \
  "buffer_load_dwordx4 v15, %[dw], %[so] offen offset:3072 lds\n\t" \
  "global_load_ushort %[gg], %[vc], %[gam]\n\t" \
  "global_load_ushort %[bb], %[vc], %[bet]\n\t" \
  "s_mov_b64 exec, 1\n\t" \
  "global_load_dword %[go], %[vo], %[out]\n\t" \
  "s_mov_b64 exec, -1\n\t" \
  "s_waitcnt vmcnt(11)\n\t" \
  "s_barrier\n\t" \
  "s_waitcnt vmcnt(3)\n\t" \
  "s_barrier\n\t" \
  "s_waitcnt vmcnt(0)\n\t" \
  "global_load_dword %[go], %[va], %[ws] offset:128 sc1\n\t" \
  "s_waitcnt vmcnt(0)\n\t" \
  ""
#define KLOOP16 \
  "v_mov_b32 v128, 0\n\t" \
  "v_mov_b32 v129, 0\n\t" \
  "v_mov_b32 v130, 0\n\t" \
  "v_mov_b32 v131, 0\n\t" \
  "s_nop 1\n\t" \
  "v_mfma_f32_32x32x16_bf16 v[64:79], v[128:131], v[128:131], 0\n\t" \
  "v_mfma_f32_32x32x16_bf16 v[80:95], v[128:131], v[128:131], 0\n\t" \
  "v_mfma_f32_32x32x16_bf16 v[96:111], v[128:131], v[128:131], 0\n\t" \
  "v_mfma_f32_32x32x16_bf16 v[112:127], v[128:131], v[128:131], 0\n\t" \
  "v_mov_b32 v208, %[cv]\n\t" \
  "v_mov_b32 v209, %[cv]\n\t" \
  "v_mov_b32 v210, %[cv]\n\t" \
  "v_mov_b32 v211, %[cv]\n\t" \
  "s_waitcnt lgkmcnt(0)\n\t" \
  "s_barrier\n\t" \
  "ds_read_b128 v[128:131], %[a0] offset:0\n\t" \
  "ds_read_b64_tr_b16 v[144:145], %[w0] offset:0\n\t" \
  "ds_read_b64_tr_b16 v[146:147], %[w1] offset:256\n\t" \
  "ds_read_b64_tr_b16 v[148:149], %[w2] offset:0\n\t" \
  "ds_read_b64_tr_b16 v[150:151], %[w3] offset:256\n\t" \
  "ds_read_b64_tr_b16 v[152:153], %[w0] offset:512\n\t" \
  "ds_read_b64_tr_b16 v[154:155], %[w1] offset:768\n\t" \
  "ds_read_b64_tr_b16 v[156:157], %[w2] offset:512\n\t" \
  "ds_read_b64_tr_b16 v[158:159], %[w3] offset:768\n\t" \
  "ds_read_b128 v[132:135], %[a0] offset:2048\n\t" \
  "ds_read_b128 v[136:139], %[a0] offset:4096\n\t" \
  "ds_read_b128 v[140:143], %[a0] offset:6144\n\t" \
  "s_cmp_eq_u64 %[cm], 0\n\t" \
  "s_cbranch_scc1 4f\n\t" \
  "s_mov_b64 exec, %[cm]\n\t" \
  "buffer_store_dwordx4 v[208:211], %[co], %[rs], 0 offen sc1\n\t" \
  "s_mov_b64 exec, -1\n\t" \
  "4:\n\t" \
  "s_waitcnt lgkmcnt(9)\n\t" \
  "v_mfma_f32_16x16x32_bf16 v[64:67], v[128:131], v[144:147], v[64:67]\n\t" \
  "ds_read_b128 v[160:163], %[a0] offset:512\n\t" \
  "s_waitcnt lgkmcnt(8)\n\t" \
  "v_mfma_f32_16x16x32_bf16 v[68:71], v[128:131], v[148:151], v[68:71]\n\t" \
  "ds_read_b64_tr_b16 v[176:177], %[w0] offset:8192\n\t" \
  "s_waitcnt lgkmcnt(7)\n\t" \
  "v_mfma_f32_16x16x32_bf16 v[72:75], v[128:131], v[152:155], v[72:75]\n\t" \
  "ds_read_b64_tr_b16 v[178:179], %[w1] offset:8448\n\t" \
  "s_waitcnt lgkmcnt(6)\n\t" \
  "v_mfma_f32_16x16x32_bf16 v[76:79], v[128:131], v[156:159], v[76:79]\n\t" \
  "ds_read_b64_tr_b16 v[180:181], %[w2] offset:8192\n\t" \
  "s_waitcnt lgkmcnt(6)\n\t" \
  "v_mfma_f32_16x16x32_bf16 v[80:83], v[132:135], v[144:147], v[80:83]\n\t" \
  "ds_read_b64_tr_b16 v[182:183], %[w3] offset:8448\n\t" \
  "v_mfma_f32_16x16x32_bf16 v[84:87], v[132:135], v[148:151], v[84:87]\n\t" \
  "ds_read_b64_tr_b16 v[184:185], %[w0] offset:8704\n\t" \
  "v_mfma_f32_16x16x32_bf16 v[88:91], v[132:135], v[152:155], v[88:91]\n\t" \
  "ds_read_b64_tr_b16 v[186:187], %[w1] offset:8960\n\t" \
  "v_mfma_f32_16x16x32_bf16 v[92:95], v[132:135], v[156:159], v[92:95]\n\t" \
  "ds_read_b64_tr_b16 v[188:189], %[w2] offset:8704\n\t" \
  "s_waitcnt lgkmcnt(8)\n\t" \
  "v_mfma_f32_16x16x32_bf16 v[96:99], v[136:139], v[144:147], v[96:99]\n\t" \
  "ds_read_b64_tr_b16 v[190:191], %[w3] offset:8960\n\t" \
  "v_mfma_f32_16x16x32_bf16 v[100:103], v[136:139], v[148:151], v[100:103]\n\t" \
  "ds_read_b128 v[164:167], %[a0] offset:2560\n\t" \
  "v_mfma_f32_16x16x32_bf16 v[104:107], v[136:139], v[152:155], v[104:107]\n\t" \
  "ds_read_b128 v[168:171], %[a0] offset:4608\n\t" \
  "v_mfma_f32_16x16x32_bf16 v[108:111], v[136:139], v[156:159], v[108:111]\n\t" \
  "ds_read_b128 v[172:175], %[a0] offset:6656\n\t" \
  "v_mfma_f32_16x16x32_bf16 v[112:115], v[140:143], v[144:147], v[112:115]\n\t" \
  "v_mfma_f32_16x16x32_bf16 v[116:119], v[140:143], v[148:151], v[116:119]\n\t" \
  "v_mfma_f32_16x16x32_bf16 v[120:123], v[140:143], v[152:155], v[120:123]\n\t" \
  "v_mfma_f32_16x16x32_bf16 v[124:127], v[140:143], v[156:159], v[124:127]\n\t" \
  "s_waitcnt lgkmcnt(3)\n\t" \
  "v_mfma_f32_16x16x32_bf16 v[64:67], v[160:163], v[176:179], v[64:67]\n\t" \
  "v_mfma_f32_16x16x32_bf16 v[68:71], v[160:163], v[180:183], v[68:71]\n\t" \
  "v_mfma_f32_16x16x32_bf16 v[72:75], v[160:163], v[184:187], v[72:75]\n\t" \
  "v_mfma_f32_16x16x32_bf16 v[76:79], v[160:163], v[188:191], v[76:79]\n\t" \
  "s_waitcnt lgkmcnt(0)\n\t" \
  "v_mfma_f32_16x16x32_bf16 v[80:83], v[164:167], v[176:179], v[80:83]\n\t" \
  "v_mfma_f32_16x16x32_bf16 v[84:87], v[164:167], v[180:183], v[84:87]\n\t" \
  "v_mfma_f32_16x16x32_bf16 v[88:91], v[164:167], v[184:187], v[88:91]\n\t" \
  "s_barrier\n\t" \
  "v_mfma_f32_16x16x32_bf16 v[92:95], v[164:167], v[188:191], v[92:95]\n\t" \
  "ds_read_b128 v[128:131], %[a1] offset:0\n\t" \
  "ds_read_b64_tr_b16 v[144:145], %[w0] offset:16384\n\t" \
  "v_mfma_f32_16x16x32_bf16 v[96:99], v[168:171], v[176:179], v[96:99]\n\t" \
  "ds_read_b64_tr_b16 v[146:147], %[w1] offset:16640\n\t" \
  "v_mfma_f32_16x16x32_bf16 v[100:103], v[168:171], v[180:183], v[100:103]\n\t" \
  "ds_read_b64_tr_b16 v[148:149], %[w2] offset:16384\n\t" \
  "v_mfma_f32_16x16x32_bf16 v[104:107], v[168:171], v[184:187], v[104:107]\n\t" \
  "ds_read_b64_tr_b16 v[150:151], %[w3] offset:16640\n\t" \
  "ds_read_b64_tr_b16 v[152:153], %[w0] offset:16896\n\t" \
  "v_mfma_f32_16x16x32_bf16 v[108:111], v[168:171], v[188:191], v[108:111]\n\t" \
  "ds_read_b64_tr_b16 v[154:155], %[w1] offset:17152\n\t" \
  "v_mfma_f32_16x16x32_bf16 v[112:115], v[172:175], v[176:179], v[112:115]\n\t" \
  "ds_read_b64_tr_b16 v[156:157], %[w2] offset:16896\n\t" \
  "v_mfma_f32_16x16x32_bf16 v[116:119], v[172:175], v[180:183], v[116:119]\n\t" \
  "ds_read_b64_tr_b16 v[158:159], %[w3] offset:17152\n\t" \
  "ds_read_b128 v[132:135], %[a1] offset:2048\n\t" \
  "v_mfma_f32_16x16x32_bf16 v[120:123], v[172:175], v[184:187], v[120:123]\n\t" \
  "ds_read_b128 v[136:139], %[a1] offset:4096\n\t" \
  "v_mfma_f32_16x16x32_bf16 v[124:127], v[172:175], v[188:191], v[124:127]\n\t" \
  "ds_read_b128 v[140:143], %[a1] offset:6144\n\t" \
  "s_waitcnt lgkmcnt(9)\n\t" \
  "v_mfma_f32_16x16x32_bf16 v[64:67], v[128:131], v[144:147], v[64:67]\n\t" \
  "ds_read_b128 v[160:163], %[a1] offset:512\n\t" \
  "s_waitcnt lgkmcnt(8)\n\t" \
  "v_mfma_f32_16x16x32_bf16 v[68:71], v[128:131], v[148:151], v[68:71]\n\t" \
  "ds_read_b64_tr_b16 v[176:177], %[w0] offset:24576\n\t" \
  "s_waitcnt lgkmcnt(7)\n\t" \
  "v_mfma_f32_16x16x32_bf16 v[72:75], v[128:131], v[152:155], v[72:75]\n\t" \
  "ds_read_b64_tr_b16 v[178:179], %[w1] offset:24832\n\t" \
  "s_waitcnt lgkmcnt(6)\n\t" \
  "v_mfma_f32_16x16x32_bf16 v[76:79], v[128:131], v[156:159], v[76:79]\n\t" \
  "ds_read_b64_tr_b16 v[180:181], %[w2] offset:24576\n\t" \
  "s_waitcnt lgkmcnt(6)\n\t" \
  "v_mfma_f32_16x16x32_bf16 v[80:83], v[132:135], v[144:147], v[80:83]\n\t" \
  "ds_read_b64_tr_b16 v[182:183], %[w3] offset:24832\n\t" \
  "v_mfma_f32_16x16x32_bf16 v[84:87], v[132:135], v[148:151], v[84:87]\n\t" \
  "ds_read_b64_tr_b16 v[184:185], %[w0] offset:25088\n\t" \
  "v_mfma_f32_16x16x32_bf16 v[88:91], v[132:135], v[152:155], v[88:91]\n\t" \
  "ds_read_b64_tr_b16 v[186:187], %[w1] offset:25344\n\t" \
  "v_mfma_f32_16x16x32_bf16 v[92:95], v[132:135], v[156:159], v[92:95]\n\t" \
  "ds_read_b64_tr_b16 v[188:189], %[w2] offset:25088\n\t" \
  "s_waitcnt lgkmcnt(8)\n\t" \
  "v_mfma_f32_16x16x32_bf16 v[96:99], v[136:139], v[144:147], v[96:99]\n\t" \
  "ds_read_b64_tr_b16 v[190:191], %[w3] offset:25344\n\t" \
  "v_mfma_f32_16x16x32_bf16 v[100:103], v[136:139], v[148:151], v[100:103]\n\t" \
  "ds_read_b128 v[164:167], %[a1] offset:2560\n\t" \
  "v_mfma_f32_16x16x32_bf16 v[104:107], v[136:139], v[152:155], v[104:107]\n\t" \
  "ds_read_b128 v[168:171], %[a1] offset:4608\n\t" \
  "v_mfma_f32_16x16x32_bf16 v[108:111], v[136:139], v[156:159], v[108:111]\n\t" \
  "ds_read_b128 v[172:175], %[a1] offset:6656\n\t" \
  "v_mfma_f32_16x16x32_bf16 v[112:115], v[140:143], v[144:147], v[112:115]\n\t" \
  "v_mfma_f32_16x16x32_bf16 v[116:119], v[140:143], v[148:151], v[116:119]\n\t" \
  "v_mfma_f32_16x16x32_bf16 v[120:123], v[140:143], v[152:155], v[120:123]\n\t" \
  "v_mfma_f32_16x16x32_bf16 v[124:127], v[140:143], v[156:159], v[124:127]\n\t" \
  "s_waitcnt lgkmcnt(3)\n\t" \
  "v_mfma_f32_16x16x32_bf16 v[64:67], v[160:163], v[176:179], v[64:67]\n\t" \
  "v_mfma_f32_16x16x32_bf16 v[68:71], v[160:163], v[180:183], v[68:71]\n\t" \
  "v_mfma_f32_16x16x32_bf16 v[72:75], v[160:163], v[184:187], v[72:75]\n\t" \
  "v_mfma_f32_16x16x32_bf16 v[76:79], v[160:163], v[188:191], v[76:79]\n\t" \
  "s_waitcnt lgkmcnt(0)\n\t" \
  "v_mfma_f32_16x16x32_bf16 v[80:83], v[164:167], v[176:179], v[80:83]\n\t" \
  "v_mfma_f32_16x16x32_bf16 v[84:87], v[164:167], v[180:183], v[84:87]\n\t" \
  "v_mfma_f32_16x16x32_bf16 v[88:91], v[164:167], v[184:187], v[88:91]\n\t" \
  "s_barrier\n\t" \
  "v_mfma_f32_16x16x32_bf16 v[92:95], v[164:167], v[188:191], v[92:95]\n\t" \
  "ds_read_b128 v[128:131], %[a0] offset:16384\n\t" \
  "ds_read_b64_tr_b16 v[144:145], %[w0] offset:32768\n\t" \
  "v_mfma_f32_16x16x32_bf16 v[96:99], v[168:171], v[176:179], v[96:99]\n\t" \
  "ds_read_b64_tr_b16 v[146:147], %[w1] offset:33024\n\t" \
  "v_mfma_f32_16x16x32_bf16 v[100:103], v[168:171], v[180:183], v[100:103]\n\t" \
  "ds_read_b64_tr_b16 v[148:149], %[w2] offset:32768\n\t" \
  "v_mfma_f32_16x16x32_bf16 v[104:107], v[168:171], v[184:187], v[104:107]\n\t" \
  "ds_read_b64_tr_b16 v[150:151], %[w3] offset:33024\n\t" \
  "ds_read_b64_tr_b16 v[152:153], %[w0] offset:33280\n\t" \
  "v_mfma_f32_16x16x32_bf16 v[108:111], v[168:171], v[188:191], v[108:111]\n\t" \
  "ds_read_b64_tr_b16 v[154:155], %[w1] offset:33536\n\t" \
  "v_mfma_f32_16x16x32_bf16 v[112:115], v[172:175], v[176:179], v[112:115]\n\t" \
  "ds_read_b64_tr_b16 v[156:157], %[w2] offset:33280\n\t" \
  "v_mfma_f32_16x16x32_bf16 v[116:119], v[172:175], v[180:183], v[116:119]\n\t" \
  "ds_read_b64_tr_b16 v[158:159], %[w3] offset:33536\n\t" \
  "ds_read_b128 v[132:135], %[a0] offset:18432\n\t" \
  "v_mfma_f32_16x16x32_bf16 v[120:123], v[172:175], v[184:187], v[120:123]\n\t" \
  "ds_read_b128 v[136:139], %[a0] offset:20480\n\t" \
  "v_mfma_f32_16x16x32_bf16 v[124:127], v[172:175], v[188:191], v[124:127]\n\t" \
  "ds_read_b128 v[140:143], %[a0] offset:22528\n\t" \
  "s_waitcnt lgkmcnt(9)\n\t" \
  "v_mfma_f32_16x16x32_bf16 v[64:67], v[128:131], v[144:147], v[64:67]\n\t" \
  "ds_read_b128 v[160:163], %[a0] offset:16896\n\t" \
  "s_waitcnt lgkmcnt(8)\n\t" \
  "v_mfma_f32_16x16x32_bf16 v[68:71], v[128:131], v[148:151], v[68:71]\n\t" \
  "s_nop 4\n\t" \
  "ds_read_b64_tr_b16 v[176:177], %[w0] offset:40960\n\t" \
  "s_waitcnt lgkmcnt(7)\n\t" \
  "v_mfma_f32_16x16x32_bf16 v[72:75], v[128:131], v[152:155], v[72:75]\n\t" \
  "ds_read_b64_tr_b16 v[178:179], %[w1] offset:41216\n\t" \
  "s_waitcnt lgkmcnt(6)\n\t" \
  "v_mfma_f32_16x16x32_bf16 v[76:79], v[128:131], v[156:159], v[76:79]\n\t" \
  "s_nop 4\n\t" \
  "ds_read_b64_tr_b16 v[180:181], %[w2] offset:40960\n\t" \
  "s_waitcnt lgkmcnt(6)\n\t" \
  "v_mfma_f32_16x16x32_bf16 v[80:83], v[132:135], v[144:147], v[80:83]\n\t" \
  "ds_read_b64_tr_b16 v[182:183], %[w3] offset:41216\n\t" \
  "v_mfma_f32_16x16x32_bf16 v[84:87], v[132:135], v[148:151], v[84:87]\n\t" \
  "s_nop 4\n\t" \
  "ds_read_b64_tr_b16 v[184:185], %[w0] offset:41472\n\t" \
  "v_mfma_f32_16x16x32_bf16 v[88:91], v[132:135], v[152:155], v[88:91]\n\t" \
  "ds_read_b64_tr_b16 v[186:187], %[w1] offset:41728\n\t" \
  "v_mfma_f32_16x16x32_bf16 v[92:95], v[132:135], v[156:159], v[92:95]\n\t" \
  "s_nop 4\n\t" \
  "ds_read_b64_tr_b16 v[188:189], %[w2] offset:41472\n\t" \
  "s_waitcnt lgkmcnt(8)\n\t" \
  "v_mfma_f32_16x16x32_bf16 v[96:99], v[136:139], v[144:147], v[96:99]\n\t" \
  "ds_read_b64_tr_b16 v[190:191], %[w3] offset:41728\n\t" \
  "v_mfma_f32_16x16x32_bf16 v[100:103], v[136:139], v[148:151], v[100:103]\n\t" \
  "s_nop 4\n\t" \
  "ds_read_b128 v[164:167], %[a0] offset:18944\n\t" \
  "v_mfma_f32_16x16x32_bf16 v[104:107], v[136:139], v[152:155], v[104:107]\n\t" \
  "ds_read_b128 v[168:171], %[a0] offset:20992\n\t" \
  "v_mfma_f32_16x16x32_bf16 v[108:111], v[136:139], v[156:159], v[108:111]\n\t" \
  "s_nop 4\n\t" \
  "ds_read_b128 v[172:175], %[a0] offset:23040\n\t" \
  "v_mfma_f32_16x16x32_bf16 v[112:115], v[140:143], v[144:147], v[112:115]\n\t" \
  "v_mfma_f32_16x16x32_bf16 v[116:119], v[140:143], v[148:151], v[116:119]\n\t" \
  "s_nop 4\n\t" \
  "v_mfma_f32_16x16x32_bf16 v[120:123], v[140:143], v[152:155], v[120:123]\n\t" \
  "v_mfma_f32_16x16x32_bf16 v[124:127], v[140:143], v[156:159], v[124:127]\n\t" \
  "s_nop 4\n\t" \
  "s_waitcnt lgkmcnt(3)\n\t" \
  "v_mfma_f32_16x16x32_bf16 v[64:67], v[160:163], v[176:179], v[64:67]\n\t" \
  "v_mfma_f32_16x16x32_bf16 v[68:71], v[160:163], v[180:183], v[68:71]\n\t" \
  "s_nop 4\n\t" \
  "v_mfma_f32_16x16x32_bf16 v[72:75], v[160:163], v[184:187], v[72:75]\n\t" \
  "v_mfma_f32_16x16x32_bf16 v[76:79], v[160:163], v[188:191], v[76:79]\n\t" \
  "s_nop 4\n\t" \
  "s_waitcnt vmcnt(0) lgkmcnt(0)\n\t" \
  "v_mfma_f32_16x16x32_bf16 v[80:83], v[164:167], v[176:179], v[80:83]\n\t" \
  "v_mfma_f32_16x16x32_bf16 v[84:87], v[164:167], v[180:183], v[84:87]\n\t" \
  "s_nop 4\n\t" \
  "v_mfma_f32_16x16x32_bf16 v[88:91], v[164:167], v[184:187], v[88:91]\n\t" \
  "s_barrier\n\t" \
  "v_mfma_f32_16x16x32_bf16 v[92:95], v[164:167], v[188:191], v[92:95]\n\t" \
  "s_nop 4\n\t" \
  "ds_read_b128 v[128:131], %[a1] offset:16384\n\t" \
  "ds_read_b64_tr_b16 v[144:145], %[w0] offset:49152\n\t" \
  "v_mfma_f32_16x16x32_bf16 v[96:99], v[168:171], v[176:179], v[96:99]\n\t" \
  "ds_read_b64_tr_b16 v[146:147], %[w1] offset:49408\n\t" \
  "v_mfma_f32_16x16x32_bf16 v[100:103], v[168:171], v[180:183], v[100:103]\n\t" \
  "s_nop 4\n\t" \
  "ds_read_b64_tr_b16 v[148:149], %[w2] offset:49152\n\t" \
  "v_mfma_f32_16x16x32_bf16 v[104:107], v[168:171], v[184:187], v[104:107]\n\t" \
  "ds_read_b64_tr_b16 v[150:151], %[w3] offset:49408\n\t" \
  "ds_read_b64_tr_b16 v[152:153], %[w0] offset:49664\n\t" \
  "v_mfma_f32_16x16x32_bf16 v[108:111], v[168:171], v[188:191], v[108:111]\n\t" \
  "s_nop 4\n\t" \
  "ds_read_b64_tr_b16 v[154:155], %[w1] offset:49920\n\t" \
  "v_mfma_f32_16x16x32_bf16 v[112:115], v[172:175], v[176:179], v[112:115]\n\t" \
  "ds_read_b64_tr_b16 v[156:157], %[w2] offset:49664\n\t" \
  "v_mfma_f32_16x16x32_bf16 v[116:119], v[172:175], v[180:183], v[116:119]\n\t" \
  "s_nop 4\n\t" \
  "ds_read_b64_tr_b16 v[158:159], %[w3] offset:49920\n\t" \
  "ds_read_b128 v[132:135], %[a1] offset:18432\n\t" \
  "v_mfma_f32_16x16x32_bf16 v[120:123], v[172:175], v[184:187], v[120:123]\n\t" \
  "ds_read_b128 v[136:139], %[a1] offset:20480\n\t" \
  "v_mfma_f32_16x16x32_bf16 v[124:127], v[172:175], v[188:191], v[124:127]\n\t" \
  "s_nop 4\n\t" \
  "ds_read_b128 v[140:143], %[a1] offset:22528\n\t" \
  "s_waitcnt lgkmcnt(9)\n\t" \
  "v_mfma_f32_16x16x32_bf16 v[64:67], v[128:131], v[144:147], v[64:67]\n\t" \
  "ds_read_b128 v[160:163], %[a1] offset:16896\n\t" \
  "s_waitcnt lgkmcnt(8)\n\t" \
  "v_mfma_f32_16x16x32_bf16 v[68:71], v[128:131], v[148:151], v[68:71]\n\t" \
  "s_nop 4\n\t" \
  "ds_read_b64_tr_b16 v[176:177], %[w0] offset:57344\n\t" \
  "s_waitcnt lgkmcnt(7)\n\t" \
  "v_mfma_f32_16x16x32_bf16 v[72:75], v[128:131], v[152:155], v[72:75]\n\t" \
  "ds_read_b64_tr_b16 v[178:179], %[w1] offset:57600\n\t" \
  "s_waitcnt lgkmcnt(6)\n\t" \
  "v_mfma_f32_16x16x32_bf16 v[76:79], v[128:131], v[156:159], v[76:79]\n\t" \
  "s_nop 4\n\t" \
  "ds_read_b64_tr_b16 v[180:181], %[w2] offset:57344\n\t" \
  "s_waitcnt lgkmcnt(6)\n\t" \
  "v_mfma_f32_16x16x32_bf16 v[80:83], v[132:135], v[144:147], v[80:83]\n\t" \
  "ds_read_b64_tr_b16 v[182:183], %[w3] offset:57600\n\t" \
  "v_mfma_f32_16x16x32_bf16 v[84:87], v[132:135], v[148:151], v[84:87]\n\t" \
  "s_nop 4\n\t" \
  "ds_read_b64_tr_b16 v[184:185], %[w0] offset:57856\n\t" \
  "v_mfma_f32_16x16x32_bf16 v[88:91], v[132:135], v[152:155], v[88:91]\n\t" \
  "ds_read_b64_tr_b16 v[186:187], %[w1] offset:58112\n\t" \
  "v_mfma_f32_16x16x32_bf16 v[92:95], v[132:135], v[156:159], v[92:95]\n\t" \
  "s_nop 4\n\t" \
  "ds_read_b64_tr_b16 v[188:189], %[w2] offset:57856\n\t" \
  "s_waitcnt lgkmcnt(8)\n\t" \
  "v_mfma_f32_16x16x32_bf16 v[96:99], v[136:139], v[144:147], v[96:99]\n\t" \
  "ds_read_b64_tr_b16 v[190:191], %[w3] offset:58112\n\t" \
  "v_mfma_f32_16x16x32_bf16 v[100:103], v[136:139], v[148:151], v[100:103]\n\t" \
  "s_nop 4\n\t" \
  "ds_read_b128 v[164:167], %[a1] offset:18944\n\t" \
  "v_mfma_f32_16x16x32_bf16 v[104:107], v[136:139], v[152:155], v[104:107]\n\t" \
  "ds_read_b128 v[168:171], %[a1] offset:20992\n\t" \
  "v_mfma_f32_16x16x32_bf16 v[108:111], v[136:139], v[156:159], v[108:111]\n\t" \
  "s_nop 4\n\t" \
  "ds_read_b128 v[172:175], %[a1] offset:23040\n\t" \
  "v_mfma_f32_16x16x32_bf16 v[112:115], v[140:143], v[144:147], v[112:115]\n\t" \
  "v_mfma_f32_16x16x32_bf16 v[116:119], v[140:143], v[148:151], v[116:119]\n\t" \
  "s_nop 4\n\t" \
  "v_mfma_f32_16x16x32_bf16 v[120:123], v[140:143], v[152:155], v[120:123]\n\t" \
  "v_mfma_f32_16x16x32_bf16 v[124:127], v[140:143], v[156:159], v[124:127]\n\t" \
  "s_nop 4\n\t" \
  "s_waitcnt lgkmcnt(3)\n\t" \
  "v_mfma_f32_16x16x32_bf16 v[64:67], v[160:163], v[176:179], v[64:67]\n\t" \
  "v_mfma_f32_16x16x32_bf16 v[68:71], v[160:163], v[180:183], v[68:71]\n\t" \
  "s_nop 4\n\t" \
  "v_mfma_f32_16x16x32_bf16 v[72:75], v[160:163], v[184:187], v[72:75]\n\t" \
  "v_mfma_f32_16x16x32_bf16 v[76:79], v[160:163], v[188:191], v[76:79]\n\t" \
  "s_nop 4\n\t" \
  "s_waitcnt lgkmcnt(0)\n\t" \
  "v_mfma_f32_16x16x32_bf16 v[80:83], v[164:167], v[176:179], v[80:83]\n\t" \
  "v_mfma_f32_16x16x32_bf16 v[84:87], v[164:167], v[180:183], v[84:87]\n\t" \
  "s_nop 4\n\t" \
  "v_mfma_f32_16x16x32_bf16 v[88:91], v[164:167], v[184:187], v[88:91]\n\t" \
  "s_barrier\n\t" \
  "v_mfma_f32_16x16x32_bf16 v[92:95], v[164:167], v[188:191], v[92:95]\n\t" \
  "s_nop 4\n\t" \
  "ds_read_b128 v[128:131], %[a0] offset:32768\n\t" \
  "ds_read_b64_tr_b16 v[144:145], %[u0] offset:0\n\t" \
  "v_mfma_f32_16x16x32_bf16 v[96:99], v[168:171], v[176:179], v[96:99]\n\t" \
  "ds_read_b64_tr_b16 v[146:147], %[u1] offset:256\n\t" \
  "v_mfma_f32_16x16x32_bf16 v[100:103], v[168:171], v[180:183], v[100:103]\n\t" \
  "s_nop 4\n\t" \
  "ds_read_b64_tr_b16 v[148:149], %[u2] offset:0\n\t" \
  "v_mfma_f32_16x16x32_bf16 v[104:107], v[168:171], v[184:187], v[104:107]\n\t" \
  "ds_read_b64_tr_b16 v[150:151], %[u3] offset:256\n\t" \
  "ds_read_b64_tr_b16 v[152:153], %[u0] offset:512\n\t" \
  "v_mfma_f32_16x16x32_bf16 v[108:111], v[168:171], v[188:191], v[108:111]\n\t" \
  "s_nop 4\n\t" \
  "ds_read_b64_tr_b16 v[154:155], %[u1] offset:768\n\t" \
  "v_mfma_f32_16x16x32_bf16 v[112:115], v[172:175], v[176:179], v[112:115]\n\t" \
  "ds_read_b64_tr_b16 v[156:157], %[u2] offset:512\n\t" \
  "v_mfma_f32_16x16x32_bf16 v[116:119], v[172:175], v[180:183], v[116:119]\n\t" \
  "s_nop 4\n\t" \
  "ds_read_b64_tr_b16 v[158:159], %[u3] offset:768\n\t" \
  "ds_read_b128 v[132:135], %[a0] offset:34816\n\t" \
  "v_mfma_f32_16x16x32_bf16 v[120:123], v[172:175], v[184:187], v[120:123]\n\t" \
  "ds_read_b128 v[136:139], %[a0] offset:36864\n\t" \
  "v_mfma_f32_16x16x32_bf16 v[124:127], v[172:175], v[188:191], v[124:127]\n\t" \
  "s_nop 4\n\t" \
  "ds_read_b128 v[140:143], %[a0] offset:38912\n\t" \
  "s_waitcnt lgkmcnt(9)\n\t" \
  "v_mfma_f32_16x16x32_bf16 v[64:67], v[128:131], v[144:147], v[64:67]\n\t" \
  "ds_read_b128 v[160:163], %[a0] offset:33280\n\t" \
  "s_waitcnt lgkmcnt(8)\n\t" \
  "v_mfma_f32_16x16x32_bf16 v[68:71], v[128:131], v[148:151], v[68:71]\n\t" \
  "s_nop 4\n\t" \
  "ds_read_b64_tr_b16 v[176:177], %[u0] offset:8192\n\t" \
  "s_waitcnt lgkmcnt(7)\n\t" \
  "v_mfma_f32_16x16x32_bf16 v[72:75], v[128:131], v[152:155], v[72:75]\n\t" \
  "ds_read_b64_tr_b16 v[178:179], %[u1] offset:8448\n\t" \
  "s_waitcnt lgkmcnt(6)\n\t" \
  "v_mfma_f32_16x16x32_bf16 v[76:79], v[128:131], v[156:159], v[76:79]\n\t" \
  "s_nop 4\n\t" \
  "ds_read_b64_tr_b16 v[180:181], %[u2] offset:8192\n\t" \
  "s_waitcnt lgkmcnt(6)\n\t" \
  "v_mfma_f32_16x16x32_bf16 v[80:83], v[132:135], v[144:147], v[80:83]\n\t" \
  "ds_read_b64_tr_b16 v[182:183], %[u3] offset:8448\n\t" \
  "v_mfma_f32_16x16x32_bf16 v[84:87], v[132:135], v[148:151], v[84:87]\n\t" \
  "s_nop 4\n\t" \
  "ds_read_b64_tr_b16 v[184:185], %[u0] offset:8704\n\t" \
  "v_mfma_f32_16x16x32_bf16 v[88:91], v[132:135], v[152:155], v[88:91]\n\t" \
  "ds_read_b64_tr_b16 v[186:187], %[u1] offset:8960\n\t" \
  "v_mfma_f32_16x16x32_bf16 v[92:95], v[132:135], v[156:159], v[92:95]\n\t" \
  "s_nop 4\n\t" \
  "ds_read_b64_tr_b16 v[188:189], %[u2] offset:8704\n\t" \
  "s_waitcnt lgkmcnt(8)\n\t" \
  "v_mfma_f32_16x16x32_bf16 v[96:99], v[136:139], v[144:147], v[96:99]\n\t" \
  "ds_read_b64_tr_b16 v[190:191], %[u3] offset:8960\n\t" \
  "v_mfma_f32_16x16x32_bf16 v[100:103], v[136:139], v[148:151], v[100:103]\n\t" \
  "s_nop 4\n\t" \
  "ds_read_b128 v[164:167], %[a0] offset:35328\n\t" \
  "v_mfma_f32_16x16x32_bf16 v[104:107], v[136:139], v[152:155], v[104:107]\n\t" \
  "ds_read_b128 v[168:171], %[a0] offset:37376\n\t" \
  "v_mfma_f32_16x16x32_bf16 v[108:111], v[136:139], v[156:159], v[108:111]\n\t" \
  "s_nop 4\n\t" \
  "ds_read_b128 v[172:175], %[a0] offset:39424\n\t" \
  "v_mfma_f32_16x16x32_bf16 v[112:115], v[140:143], v[144:147], v[112:115]\n\t" \
  "v_mfma_f32_16x16x32_bf16 v[116:119], v[140:143], v[148:151], v[116:119]\n\t" \
  "s_nop 4\n\t" \
  "v_mfma_f32_16x16x32_bf16 v[120:123], v[140:143], v[152:155], v[120:123]\n\t" \
  "v_mfma_f32_16x16x32_bf16 v[124:127], v[140:143], v[156:159], v[124:127]\n\t" \
  "s_nop 4\n\t" \
  "s_waitcnt lgkmcnt(3)\n\t" \
  "v_mfma_f32_16x16x32_bf16 v[64:67], v[160:163], v[176:179], v[64:67]\n\t" \
  "v_mfma_f32_16x16x32_bf16 v[68:71], v[160:163], v[180:183], v[68:71]\n\t" \
  "s_nop 4\n\t" \
  "v_mfma_f32_16x16x32_bf16 v[72:75], v[160:163], v[184:187], v[72:75]\n\t" \
  "v_mfma_f32_16x16x32_bf16 v[76:79], v[160:163], v[188:191], v[76:79]\n\t" \
  "s_nop 4\n\t" \
  "s_waitcnt lgkmcnt(0)\n\t" \
  "v_mfma_f32_16x16x32_bf16 v[80:83], v[164:167], v[176:179], v[80:83]\n\t" \
  "v_mfma_f32_16x16x32_bf16 v[84:87], v[164:167], v[180:183], v[84:87]\n\t" \
  "s_nop 4\n\t" \
  "v_mfma_f32_16x16x32_bf16 v[88:91], v[164:167], v[184:187], v[88:91]\n\t" \
  "s_barrier\n\t" \
  "v_mfma_f32_16x16x32_bf16 v[92:95], v[164:167], v[188:191], v[92:95]\n\t" \
  "s_nop 4\n\t" \
  "ds_read_b128 v[128:131], %[a0] offset:49152\n\t" \
  "ds_read_b64_tr_b16 v[144:145], %[u0] offset:16384\n\t" \
  "v_mfma_f32_16x16x32_bf16 v[96:99], v[168:171], v[176:179], v[96:99]\n\t" \
  "ds_read_b64_tr_b16 v[146:147], %[u1] offset:16640\n\t" \
  "v_mfma_f32_16x16x32_bf16 v[100:103], v[168:171], v[180:183], v[100:103]\n\t" \
  "s_nop 4\n\t" \
  "ds_read_b64_tr_b16 v[148:149], %[u2] offset:16384\n\t" \
  "v_mfma_f32_16x16x32_bf16 v[104:107], v[168:171], v[184:187], v[104:107]\n\t" \
  "ds_read_b64_tr_b16 v[150:151], %[u3] offset:16640\n\t" \
  "ds_read_b64_tr_b16 v[152:153], %[u0] offset:16896\n\t" \
  "v_mfma_f32_16x16x32_bf16 v[108:111], v[168:171], v[188:191], v[108:111]\n\t" \
  "s_nop 4\n\t" \
  "ds_read_b64_tr_b16 v[154:155], %[u1] offset:17152\n\t" \
  "v_mfma_f32_16x16x32_bf16 v[112:115], v[172:175], v[176:179], v[112:115]\n\t" \
  "ds_read_b64_tr_b16 v[156:157], %[u2] offset:16896\n\t" \
  "v_mfma_f32_16x16x32_bf16 v[116:119], v[172:175], v[180:183], v[116:119]\n\t" \
  "s_nop 4\n\t" \
  "ds_read_b64_tr_b16 v[158:159], %[u3] offset:17152\n\t" \
  "ds_read_b128 v[132:135], %[a0] offset:51200\n\t" \
  "v_mfma_f32_16x16x32_bf16 v[120:123], v[172:175], v[184:187], v[120:123]\n\t" \
  "ds_read_b128 v[136:139], %[a0] offset:53248\n\t" \
  "v_mfma_f32_16x16x32_bf16 v[124:127], v[172:175], v[188:191], v[124:127]\n\t" \
  "s_nop 4\n\t" \
  "ds_read_b128 v[140:143], %[a0] offset:55296\n\t" \
  "s_waitcnt lgkmcnt(9)\n\t" \
  "v_mfma_f32_16x16x32_bf16 v[64:67], v[128:131], v[144:147], v[64:67]\n\t" \
  "ds_read_b128 v[160:163], %[a0] offset:49664\n\t" \
  "s_waitcnt lgkmcnt(8)\n\t" \
  "v_mfma_f32_16x16x32_bf16 v[68:71], v[128:131], v[148:151], v[68:71]\n\t" \
  "s_nop 4\n\t" \
  "ds_read_b64_tr_b16 v[176:177], %[u0] offset:24576\n\t" \
  "s_waitcnt lgkmcnt(7)\n\t" \
  "v_mfma_f32_16x16x32_bf16 v[72:75], v[128:131], v[152:155], v[72:75]\n\t" \
  "ds_read_b64_tr_b16 v[178:179], %[u1] offset:24832\n\t" \
  "s_waitcnt lgkmcnt(6)\n\t" \
  "v_mfma_f32_16x16x32_bf16 v[76:79], v[128:131], v[156:159], v[76:79]\n\t" \
  "s_nop 4\n\t" \
  "ds_read_b64_tr_b16 v[180:181], %[u2] offset:24576\n\t" \
  "s_waitcnt lgkmcnt(6)\n\t" \
  "v_mfma_f32_16x16x32_bf16 v[80:83], v[132:135], v[144:147], v[80:83]\n\t" \
  "ds_read_b64_tr_b16 v[182:183], %[u3] offset:24832\n\t" \
  "v_mfma_f32_16x16x32_bf16 v[84:87], v[132:135], v[148:151], v[84:87]\n\t" \
  "s_nop 4\n\t" \
  "ds_read_b64_tr_b16 v[184:185], %[u0] offset:25088\n\t" \
  "v_mfma_f32_16x16x32_bf16 v[88:91], v[132:135], v[152:155], v[88:91]\n\t" \
  "ds_read_b64_tr_b16 v[186:187], %[u1] offset:25344\n\t" \
  "v_mfma_f32_16x16x32_bf16 v[92:95], v[132:135], v[156:159], v[92:95]\n\t" \
  "s_nop 4\n\t" \
  "ds_read_b64_tr_b16 v[188:189], %[u2] offset:25088\n\t" \
  "s_waitcnt lgkmcnt(8)\n\t" \
  "v_mfma_f32_16x16x32_bf16 v[96:99], v[136:139], v[144:147], v[96:99]\n\t" \
  "ds_read_b64_tr_b16 v[190:191], %[u3] offset:25344\n\t" \
  "v_mfma_f32_16x16x32_bf16 v[100:103], v[136:139], v[148:151], v[100:103]\n\t" \
  "s_nop 4\n\t" \
  "ds_read_b128 v[164:167], %[a0] offset:51712\n\t" \
  "v_mfma_f32_16x16x32_bf16 v[104:107], v[136:139], v[152:155], v[104:107]\n\t" \
  "ds_read_b128 v[168:171], %[a0] offset:53760\n\t" \
  "v_mfma_f32_16x16x32_bf16 v[108:111], v[136:139], v[156:159], v[108:111]\n\t" \
  "s_nop 4\n\t" \
  "ds_read_b128 v[172:175], %[a0] offset:55808\n\t" \
  "v_mfma_f32_16x16x32_bf16 v[112:115], v[140:143], v[144:147], v[112:115]\n\t" \
  "v_mfma_f32_16x16x32_bf16 v[116:119], v[140:143], v[148:151], v[116:119]\n\t" \
  "s_nop 4\n\t" \
  "v_mfma_f32_16x16x32_bf16 v[120:123], v[140:143], v[152:155], v[120:123]\n\t" \
  "v_mfma_f32_16x16x32_bf16 v[124:127], v[140:143], v[156:159], v[124:127]\n\t" \
  "s_nop 4\n\t" \
  "s_waitcnt lgkmcnt(3)\n\t" \
  "v_mfma_f32_16x16x32_bf16 v[64:67], v[160:163], v[176:179], v[64:67]\n\t" \
  "v_mfma_f32_16x16x32_bf16 v[68:71], v[160:163], v[180:183], v[68:71]\n\t" \
  "s_nop 4\n\t" \
  "v_mfma_f32_16x16x32_bf16 v[72:75], v[160:163], v[184:187], v[72:75]\n\t" \
  "v_mfma_f32_16x16x32_bf16 v[76:79], v[160:163], v[188:191], v[76:79]\n\t" \
  "s_nop 4\n\t" \
  "s_waitcnt lgkmcnt(0)\n\t" \
  "v_mfma_f32_16x16x32_bf16 v[80:83], v[164:167], v[176:179], v[80:83]\n\t" \
  "v_mfma_f32_16x16x32_bf16 v[84:87], v[164:167], v[180:183], v[84:87]\n\t" \
  "s_nop 4\n\t" \
  "v_mfma_f32_16x16x32_bf16 v[88:91], v[164:167], v[184:187], v[88:91]\n\t" \
  "s_barrier\n\t" \
  "v_mfma_f32_16x16x32_bf16 v[92:95], v[164:167], v[188:191], v[92:95]\n\t" \
  "s_nop 4\n\t" \
  "ds_read_b128 v[128:131], %[a0] offset:0\n\t" \
  "ds_read_b64_tr_b16 v[144:145], %[w0] offset:0\n\t" \
  "v_mfma_f32_16x16x32_bf16 v[96:99], v[168:171], v[176:179], v[96:99]\n\t" \
  "ds_read_b64_tr_b16 v[146:147], %[w1] offset:256\n\t" \
  "v_mfma_f32_16x16x32_bf16 v[100:103], v[168:171], v[180:183], v[100:103]\n\t" \
  "s_nop 4\n\t" \
  "ds_read_b64_tr_b16 v[148:149], %[w2] offset:0\n\t" \
  "v_mfma_f32_16x16x32_bf16 v[104:107], v[168:171], v[184:187], v[104:107]\n\t" \
  "ds_read_b64_tr_b16 v[150:151], %[w3] offset:256\n\t" \
  "ds_read_b64_tr_b16 v[152:153], %[w0] offset:512\n\t" \
  "v_mfma_f32_16x16x32_bf16 v[108:111], v[168:171], v[188:191], v[108:111]\n\t" \
  "s_nop 4\n\t" \
  "ds_read_b64_tr_b16 v[154:155], %[w1] offset:768\n\t" \
  "v_mfma_f32_16x16x32_bf16 v[112:115], v[172:175], v[176:179], v[112:115]\n\t" \
  "ds_read_b64_tr_b16 v[156:157], %[w2] offset:512\n\t" \
  "v_mfma_f32_16x16x32_bf16 v[116:119], v[172:175], v[180:183], v[116:119]\n\t" \
  "s_nop 4\n\t" \
  "ds_read_b64_tr_b16 v[158:159], %[w3] offset:768\n\t" \
  "ds_read_b128 v[132:135], %[a0] offset:2048\n\t" \
  "v_mfma_f32_16x16x32_bf16 v[120:123], v[172:175], v[184:187], v[120:123]\n\t" \
  "ds_read_b128 v[136:139], %[a0] offset:4096\n\t" \
  "v_mfma_f32_16x16x32_bf16 v[124:127], v[172:175], v[188:191], v[124:127]\n\t" \
  "s_nop 4\n\t" \
  "ds_read_b128 v[140:143], %[a0] offset:6144\n\t" \
  "s_waitcnt lgkmcnt(9)\n\t" \
  "v_mfma_f32_16x16x32_bf16 v[64:67], v[128:131], v[144:147], v[64:67]\n\t" \
  "ds_read_b128 v[160:163], %[a0] offset:512\n\t" \
  "s_waitcnt lgkmcnt(8)\n\t" \
  "v_mfma_f32_16x16x32_bf16 v[68:71], v[128:131], v[148:151], v[68:71]\n\t" \
  "s_nop 4\n\t" \
  "ds_read_b64_tr_b16 v[176:177], %[w0] offset:8192\n\t" \
  "s_waitcnt lgkmcnt(7)\n\t" \
  "v_mfma_f32_16x16x32_bf16 v[72:75], v[128:131], v[152:155], v[72:75]\n\t" \
  "ds_read_b64_tr_b16 v[178:179], %[w1] offset:8448\n\t" \
  "s_waitcnt lgkmcnt(6)\n\t" \
  "v_mfma_f32_16x16x32_bf16 v[76:79], v[128:131], v[156:159], v[76:79]\n\t" \
  "s_nop 4\n\t" \
  "ds_read_b64_tr_b16 v[180:181], %[w2] offset:8192\n\t" \
  "s_waitcnt lgkmcnt(6)\n\t" \
  "v_mfma_f32_16x16x32_bf16 v[80:83], v[132:135], v[144:147], v[80:83]\n\t" \
  "ds_read_b64_tr_b16 v[182:183], %[w3] offset:8448\n\t" \
  "v_mfma_f32_16x16x32_bf16 v[84:87], v[132:135], v[148:151], v[84:87]\n\t" \
  "s_nop 4\n\t" \
  "ds_read_b64_tr_b16 v[184:185], %[w0] offset:8704\n\t" \
  "v_mfma_f32_16x16x32_bf16 v[88:91], v[132:135], v[152:155], v[88:91]\n\t" \
  "ds_read_b64_tr_b16 v[186:187], %[w1] offset:8960\n\t" \
  "v_mfma_f32_16x16x32_bf16 v[92:95], v[132:135], v[156:159], v[92:95]\n\t" \
  "s_nop 4\n\t" \
  "ds_read_b64_tr_b16 v[188:189], %[w2] offset:8704\n\t" \
  "s_waitcnt lgkmcnt(8)\n\t" \
  "v_mfma_f32_16x16x32_bf16 v[96:99], v[136:139], v[144:147], v[96:99]\n\t" \
  "ds_read_b64_tr_b16 v[190:191], %[w3] offset:8960\n\t" \
  "v_mfma_f32_16x16x32_bf16 v[100:103], v[136:139], v[148:151], v[100:103]\n\t" \
  "s_nop 4\n\t" \
  "ds_read_b128 v[164:167], %[a0] offset:2560\n\t" \
  "v_mfma_f32_16x16x32_bf16 v[104:107], v[136:139], v[152:155], v[104:107]\n\t" \
  "ds_read_b128 v[168:171], %[a0] offset:4608\n\t" \
  "v_mfma_f32_16x16x32_bf16 v[108:111], v[136:139], v[156:159], v[108:111]\n\t" \
  "s_nop 4\n\t" \
  "ds_read_b128 v[172:175], %[a0] offset:6656\n\t" \
  "v_mfma_f32_16x16x32_bf16 v[112:115], v[140:143], v[144:147], v[112:115]\n\t" \
  "v_mfma_f32_16x16x32_bf16 v[116:119], v[140:143], v[148:151], v[116:119]\n\t" \
  "s_nop 4\n\t" \
  "v_mfma_f32_16x16x32_bf16 v[120:123], v[140:143], v[152:155], v[120:123]\n\t" \
  "v_mfma_f32_16x16x32_bf16 v[124:127], v[140:143], v[156:159], v[124:127]\n\t" \
  "s_nop 4\n\t" \
  "s_waitcnt lgkmcnt(3)\n\t" \
  "v_mfma_f32_16x16x32_bf16 v[64:67], v[160:163], v[176:179], v[64:67]\n\t" \
  "v_mfma_f32_16x16x32_bf16 v[68:71], v[160:163], v[180:183], v[68:71]\n\t" \
  "s_nop 4\n\t" \
  "v_mfma_f32_16x16x32_bf16 v[72:75], v[160:163], v[184:187], v[72:75]\n\t" \
  "v_mfma_f32_16x16x32_bf16 v[76:79], v[160:163], v[188:191], v[76:79]\n\t" \
  "s_nop 4\n\t" \
  "s_waitcnt lgkmcnt(0)\n\t" \
  "v_mfma_f32_16x16x32_bf16 v[80:83], v[164:167], v[176:179], v[80:83]\n\t" \
  "v_mfma_f32_16x16x32_bf16 v[84:87], v[164:167], v[180:183], v[84:87]\n\t" \
  "s_nop 4\n\t" \
  "v_mfma_f32_16x16x32_bf16 v[88:91], v[164:167], v[184:187], v[88:91]\n\t" \
  "s_barrier\n\t" \
  "v_mfma_f32_16x16x32_bf16 v[92:95], v[164:167], v[188:191], v[92:95]\n\t" \
  "s_nop 4\n\t" \
  "ds_read_b128 v[128:131], %[a1] offset:0\n\t" \
  "ds_read_b64_tr_b16 v[144:145], %[w0] offset:16384\n\t" \
  "v_mfma_f32_16x16x32_bf16 v[96:99], v[168:171], v[176:179], v[96:99]\n\t" \
  "ds_read_b64_tr_b16 v[146:147], %[w1] offset:16640\n\t" \
  "v_mfma_f32_16x16x32_bf16 v[100:103], v[168:171], v[180:183], v[100:103]\n\t" \
  "s_nop 4\n\t" \
  "ds_read_b64_tr_b16 v[148:149], %[w2] offset:16384\n\t" \
  "v_mfma_f32_16x16x32_bf16 v[104:107], v[168:171], v[184:187], v[104:107]\n\t" \
  "ds_read_b64_tr_b16 v[150:151], %[w3] offset:16640\n\t" \
  "ds_read_b64_tr_b16 v[152:153], %[w0] offset:16896\n\t" \
  "v_mfma_f32_16x16x32_bf16 v[108:111], v[168:171], v[188:191], v[108:111]\n\t" \
  "s_nop 4\n\t" \
  "ds_read_b64_tr_b16 v[154:155], %[w1] offset:17152\n\t" \
  "v_mfma_f32_16x16x32_bf16 v[112:115], v[172:175], v[176:179], v[112:115]\n\t" \
  "ds_read_b64_tr_b16 v[156:157], %[w2] offset:16896\n\t" \
  "v_mfma_f32_16x16x32_bf16 v[116:119], v[172:175], v[180:183], v[116:119]\n\t" \
  "s_nop 4\n\t" \
  "ds_read_b64_tr_b16 v[158:159], %[w3] offset:17152\n\t" \
  "ds_read_b128 v[132:135], %[a1] offset:2048\n\t" \
  "v_mfma_f32_16x16x32_bf16 v[120:123], v[172:175], v[184:187], v[120:123]\n\t" \
  "ds_read_b128 v[136:139], %[a1] offset:4096\n\t" \
  "v_mfma_f32_16x16x32_bf16 v[124:127], v[172:175], v[188:191], v[124:127]\n\t" \
  "s_nop 4\n\t" \
  "ds_read_b128 v[140:143], %[a1] offset:6144\n\t" \
  "s_waitcnt lgkmcnt(9)\n\t" \
  "v_mfma_f32_16x16x32_bf16 v[64:67], v[128:131], v[144:147], v[64:67]\n\t" \
  "ds_read_b128 v[160:163], %[a1] offset:512\n\t" \
  "s_waitcnt lgkmcnt(8)\n\t" \
  "v_mfma_f32_16x16x32_bf16 v[68:71], v[128:131], v[148:151], v[68:71]\n\t" \
  "ds_read_b64_tr_b16 v[176:177], %[w0] offset:24576\n\t" \
  "s_waitcnt lgkmcnt(7)\n\t" \
  "v_mfma_f32_16x16x32_bf16 v[72:75], v[128:131], v[152:155], v[72:75]\n\t" \
  "ds_read_b64_tr_b16 v[178:179], %[w1] offset:24832\n\t" \
  "s_waitcnt lgkmcnt(6)\n\t" \
  "v_mfma_f32_16x16x32_bf16 v[76:79], v[128:131], v[156:159], v[76:79]\n\t" \
  "ds_read_b64_tr_b16 v[180:181], %[w2] offset:24576\n\t" \
  "s_waitcnt lgkmcnt(6)\n\t" \
  "v_mfma_f32_16x16x32_bf16 v[80:83], v[132:135], v[144:147], v[80:83]\n\t" \
  "ds_read_b64_tr_b16 v[182:183], %[w3] offset:24832\n\t" \
  "v_mfma_f32_16x16x32_bf16 v[84:87], v[132:135], v[148:151], v[84:87]\n\t" \
  "ds_read_b64_tr_b16 v[184:185], %[w0] offset:25088\n\t" \
  "v_mfma_f32_16x16x32_bf16 v[88:91], v[132:135], v[152:155], v[88:91]\n\t" \
  "ds_read_b64_tr_b16 v[186:187], %[w1] offset:25344\n\t" \
  "v_mfma_f32_16x16x32_bf16 v[92:95], v[132:135], v[156:159], v[92:95]\n\t" \
  "ds_read_b64_tr_b16 v[188:189], %[w2] offset:25088\n\t" \
  "s_waitcnt lgkmcnt(8)\n\t" \
  "v_mfma_f32_16x16x32_bf16 v[96:99], v[136:139], v[144:147], v[96:99]\n\t" \
  "ds_read_b64_tr_b16 v[190:191], %[w3] offset:25344\n\t" \
  "v_mfma_f32_16x16x32_bf16 v[100:103], v[136:139], v[148:151], v[100:103]\n\t" \
  "ds_read_b128 v[164:167], %[a1] offset:2560\n\t" \
  "v_mfma_f32_16x16x32_bf16 v[104:107], v[136:139], v[152:155], v[104:107]\n\t" \
  "ds_read_b128 v[168:171], %[a1] offset:4608\n\t" \
  "v_mfma_f32_16x16x32_bf16 v[108:111], v[136:139], v[156:159], v[108:111]\n\t" \
  "ds_read_b128 v[172:175], %[a1] offset:6656\n\t" \
  "v_mfma_f32_16x16x32_bf16 v[112:115], v[140:143], v[144:147], v[112:115]\n\t" \
  "v_mfma_f32_16x16x32_bf16 v[116:119], v[140:143], v[148:151], v[116:119]\n\t" \
  "v_mfma_f32_16x16x32_bf16 v[120:123], v[140:143], v[152:155], v[120:123]\n\t" \
  "v_mfma_f32_16x16x32_bf16 v[124:127], v[140:143], v[156:159], v[124:127]\n\t" \
  "s_waitcnt lgkmcnt(3)\n\t" \
  "v_mfma_f32_16x16x32_bf16 v[64:67], v[160:163], v[176:179], v[64:67]\n\t" \
  "v_mfma_f32_16x16x32_bf16 v[68:71], v[160:163], v[180:183], v[68:71]\n\t" \
  "v_mfma_f32_16x16x32_bf16 v[72:75], v[160:163], v[184:187], v[72:75]\n\t" \
  "v_mfma_f32_16x16x32_bf16 v[76:79], v[160:163], v[188:191], v[76:79]\n\t" \
  "s_waitcnt lgkmcnt(0)\n\t" \
  "v_mfma_f32_16x16x32_bf16 v[80:83], v[164:167], v[176:179], v[80:83]\n\t" \
  "v_mfma_f32_16x16x32_bf16 v[84:87], v[164:167], v[180:183], v[84:87]\n\t" \
  "v_mfma_f32_16x16x32_bf16 v[88:91], v[164:167], v[184:187], v[88:91]\n\t" \
  "s_barrier\n\t" \
  "v_mfma_f32_16x16x32_bf16 v[92:95], v[164:167], v[188:191], v[92:95]\n\t" \
  "ds_read_b128 v[128:131], %[a0] offset:16384\n\t" \
  "ds_read_b64_tr_b16 v[144:145], %[w0] offset:32768\n\t" \
  "v_mfma_f32_16x16x32_bf16 v[96:99], v[168:171], v[176:179], v[96:99]\n\t" \
  "ds_read_b64_tr_b16 v[146:147], %[w1] offset:33024\n\t" \
  "v_mfma_f32_16x16x32_bf16 v[100:103], v[168:171], v[180:183], v[100:103]\n\t" \
  "ds_read_b64_tr_b16 v[148:149], %[w2] offset:32768\n\t" \
  "v_mfma_f32_16x16x32_bf16 v[104:107], v[168:171], v[184:187], v[104:107]\n\t" \
  "ds_read_b64_tr_b16 v[150:151], %[w3] offset:33024\n\t" \
  "ds_read_b64_tr_b16 v[152:153], %[w0] offset:33280\n\t" \
  "v_mfma_f32_16x16x32_bf16 v[108:111], v[168:171], v[188:191], v[108:111]\n\t" \
  "ds_read_b64_tr_b16 v[154:155], %[w1] offset:33536\n\t" \
  "v_mfma_f32_16x16x32_bf16 v[112:115], v[172:175], v[176:179], v[112:115]\n\t" \
  "ds_read_b64_tr_b16 v[156:157], %[w2] offset:33280\n\t" \
  "v_mfma_f32_16x16x32_bf16 v[116:119], v[172:175], v[180:183], v[116:119]\n\t" \
  "ds_read_b64_tr_b16 v[158:159], %[w3] offset:33536\n\t" \
  "ds_read_b128 v[132:135], %[a0] offset:18432\n\t" \
  "v_mfma_f32_16x16x32_bf16 v[120:123], v[172:175], v[184:187], v[120:123]\n\t" \
  "ds_read_b128 v[136:139], %[a0] offset:20480\n\t" \
  "v_mfma_f32_16x16x32_bf16 v[124:127], v[172:175], v[188:191], v[124:127]\n\t" \
  "ds_read_b128 v[140:143], %[a0] offset:22528\n\t" \
  "s_waitcnt lgkmcnt(9)\n\t" \
  "v_mfma_f32_16x16x32_bf16 v[64:67], v[128:131], v[144:147], v[64:67]\n\t" \
  "ds_read_b128 v[160:163], %[a0] offset:16896\n\t" \
  "s_waitcnt lgkmcnt(8)\n\t" \
  "v_mfma_f32_16x16x32_bf16 v[68:71], v[128:131], v[148:151], v[68:71]\n\t" \
  "ds_read_b64_tr_b16 v[176:177], %[w0] offset:40960\n\t" \
  "s_waitcnt lgkmcnt(7)\n\t" \
  "v_mfma_f32_16x16x32_bf16 v[72:75], v[128:131], v[152:155], v[72:75]\n\t" \
  "ds_read_b64_tr_b16 v[178:179], %[w1] offset:41216\n\t" \
  "s_waitcnt lgkmcnt(6)\n\t" \
  "v_mfma_f32_16x16x32_bf16 v[76:79], v[128:131], v[156:159], v[76:79]\n\t" \
  "ds_read_b64_tr_b16 v[180:181], %[w2] offset:40960\n\t" \
  "s_waitcnt lgkmcnt(6)\n\t" \
  "v_mfma_f32_16x16x32_bf16 v[80:83], v[132:135], v[144:147], v[80:83]\n\t" \
  "ds_read_b64_tr_b16 v[182:183], %[w3] offset:41216\n\t" \
  "v_mfma_f32_16x16x32_bf16 v[84:87], v[132:135], v[148:151], v[84:87]\n\t" \
  "ds_read_b64_tr_b16 v[184:185], %[w0] offset:41472\n\t" \
  "v_mfma_f32_16x16x32_bf16 v[88:91], v[132:135], v[152:155], v[88:91]\n\t" \
  "ds_read_b64_tr_b16 v[186:187], %[w1] offset:41728\n\t" \
  "v_mfma_f32_16x16x32_bf16 v[92:95], v[132:135], v[156:159], v[92:95]\n\t" \
  "ds_read_b64_tr_b16 v[188:189], %[w2] offset:41472\n\t" \
  "s_waitcnt lgkmcnt(8)\n\t" \
  "v_mfma_f32_16x16x32_bf16 v[96:99], v[136:139], v[144:147], v[96:99]\n\t" \
  "ds_read_b64_tr_b16 v[190:191], %[w3] offset:41728\n\t" \
  "v_mfma_f32_16x16x32_bf16 v[100:103], v[136:139], v[148:151], v[100:103]\n\t" \
  "ds_read_b128 v[164:167], %[a0] offset:18944\n\t" \
  "v_mfma_f32_16x16x32_bf16 v[104:107], v[136:139], v[152:155], v[104:107]\n\t" \
  "ds_read_b128 v[168:171], %[a0] offset:20992\n\t" \
  "v_mfma_f32_16x16x32_bf16 v[108:111], v[136:139], v[156:159], v[108:111]\n\t" \
  "ds_read_b128 v[172:175], %[a0] offset:23040\n\t" \
  "v_mfma_f32_16x16x32_bf16 v[112:115], v[140:143], v[144:147], v[112:115]\n\t" \
  "v_mfma_f32_16x16x32_bf16 v[116:119], v[140:143], v[148:151], v[116:119]\n\t" \
  "v_mfma_f32_16x16x32_bf16 v[120:123], v[140:143], v[152:155], v[120:123]\n\t" \
  "v_mfma_f32_16x16x32_bf16 v[124:127], v[140:143], v[156:159], v[124:127]\n\t" \
  "s_waitcnt lgkmcnt(3)\n\t" \
  "v_mfma_f32_16x16x32_bf16 v[64:67], v[160:163], v[176:179], v[64:67]\n\t" \
  "v_mfma_f32_16x16x32_bf16 v[68:71], v[160:163], v[180:183], v[68:71]\n\t" \
  "v_mfma_f32_16x16x32_bf16 v[72:75], v[160:163], v[184:187], v[72:75]\n\t" \
  "v_mfma_f32_16x16x32_bf16 v[76:79], v[160:163], v[188:191], v[76:79]\n\t" \
  "s_waitcnt lgkmcnt(0)\n\t" \
  "v_mfma_f32_16x16x32_bf16 v[80:83], v[164:167], v[176:179], v[80:83]\n\t" \
  "v_mfma_f32_16x16x32_bf16 v[84:87], v[164:167], v[180:183], v[84:87]\n\t" \
  "v_mfma_f32_16x16x32_bf16 v[88:91], v[164:167], v[184:187], v[88:91]\n\t" \
  "s_barrier\n\t" \
  "v_mfma_f32_16x16x32_bf16 v[92:95], v[164:167], v[188:191], v[92:95]\n\t" \
  "ds_read_b128 v[128:131], %[a1] offset:16384\n\t" \
  "ds_read_b64_tr_b16 v[144:145], %[w0] offset:49152\n\t" \
  "v_mfma_f32_16x16x32_bf16 v[96:99], v[168:171], v[176:179], v[96:99]\n\t" \
  "ds_read_b64_tr_b16 v[146:147], %[w1] offset:49408\n\t" \
  "v_mfma_f32_16x16x32_bf16 v[100:103], v[168:171], v[180:183], v[100:103]\n\t" \
  "ds_read_b64_tr_b16 v[148:149], %[w2] offset:49152\n\t" \
  "v_mfma_f32_16x16x32_bf16 v[104:107], v[168:171], v[184:187], v[104:107]\n\t" \
  "ds_read_b64_tr_b16 v[150:151], %[w3] offset:49408\n\t" \
  "ds_read_b64_tr_b16 v[152:153], %[w0] offset:49664\n\t" \
  "v_mfma_f32_16x16x32_bf16 v[108:111], v[168:171], v[188:191], v[108:111]\n\t" \
  "ds_read_b64_tr_b16 v[154:155], %[w1] offset:49920\n\t" \
  "v_mfma_f32_16x16x32_bf16 v[112:115], v[172:175], v[176:179], v[112:115]\n\t" \
  "ds_read_b64_tr_b16 v[156:157], %[w2] offset:49664\n\t" \
  "v_mfma_f32_16x16x32_bf16 v[116:119], v[172:175], v[180:183], v[116:119]\n\t" \
  "ds_read_b64_tr_b16 v[158:159], %[w3] offset:49920\n\t" \
  "ds_read_b128 v[132:135], %[a1] offset:18432\n\t" \
  "v_mfma_f32_16x16x32_bf16 v[120:123], v[172:175], v[184:187], v[120:123]\n\t" \
  "ds_read_b128 v[136:139], %[a1] offset:20480\n\t" \
  "v_mfma_f32_16x16x32_bf16 v[124:127], v[172:175], v[188:191], v[124:127]\n\t" \
  "ds_read_b128 v[140:143], %[a1] offset:22528\n\t" \
  "s_waitcnt lgkmcnt(9)\n\t" \
  "v_mfma_f32_16x16x32_bf16 v[64:67], v[128:131], v[144:147], v[64:67]\n\t" \
  "ds_read_b128 v[160:163], %[a1] offset:16896\n\t" \
  "s_waitcnt lgkmcnt(8)\n\t" \
  "v_mfma_f32_16x16x32_bf16 v[68:71], v[128:131], v[148:151], v[68:71]\n\t" \
  "ds_read_b64_tr_b16 v[176:177], %[w0] offset:57344\n\t" \
  "s_waitcnt lgkmcnt(7)\n\t" \
  "v_mfma_f32_16x16x32_bf16 v[72:75], v[128:131], v[152:155], v[72:75]\n\t" \
  "ds_read_b64_tr_b16 v[178:179], %[w1] offset:57600\n\t" \
  "s_waitcnt lgkmcnt(6)\n\t" \
  "v_mfma_f32_16x16x32_bf16 v[76:79], v[128:131], v[156:159], v[76:79]\n\t" \
  "ds_read_b64_tr_b16 v[180:181], %[w2] offset:57344\n\t" \
  "s_waitcnt lgkmcnt(6)\n\t" \
  "v_mfma_f32_16x16x32_bf16 v[80:83], v[132:135], v[144:147], v[80:83]\n\t" \
  "ds_read_b64_tr_b16 v[182:183], %[w3] offset:57600\n\t" \
  "v_mfma_f32_16x16x32_bf16 v[84:87], v[132:135], v[148:151], v[84:87]\n\t" \
  "ds_read_b64_tr_b16 v[184:185], %[w0] offset:57856\n\t" \
  "v_mfma_f32_16x16x32_bf16 v[88:91], v[132:135], v[152:155], v[88:91]\n\t" \
  "ds_read_b64_tr_b16 v[186:187], %[w1] offset:58112\n\t" \
  "v_mfma_f32_16x16x32_bf16 v[92:95], v[132:135], v[156:159], v[92:95]\n\t" \
  "ds_read_b64_tr_b16 v[188:189], %[w2] offset:57856\n\t" \
  "s_waitcnt lgkmcnt(8)\n\t" \
  "v_mfma_f32_16x16x32_bf16 v[96:99], v[136:139], v[144:147], v[96:99]\n\t" \
  "ds_read_b64_tr_b16 v[190:191], %[w3] offset:58112\n\t" \
  "v_mfma_f32_16x16x32_bf16 v[100:103], v[136:139], v[148:151], v[100:103]\n\t" \
  "ds_read_b128 v[164:167], %[a1] offset:18944\n\t" \
  "v_mfma_f32_16x16x32_bf16 v[104:107], v[136:139], v[152:155], v[104:107]\n\t" \
  "ds_read_b128 v[168:171], %[a1] offset:20992\n\t" \
  "v_mfma_f32_16x16x32_bf16 v[108:111], v[136:139], v[156:159], v[108:111]\n\t" \
  "ds_read_b128 v[172:175], %[a1] offset:23040\n\t" \
  "v_mfma_f32_16x16x32_bf16 v[112:115], v[140:143], v[144:147], v[112:115]\n\t" \
  "v_mfma_f32_16x16x32_bf16 v[116:119], v[140:143], v[148:151], v[116:119]\n\t" \
  "v_mfma_f32_16x16x32_bf16 v[120:123], v[140:143], v[152:155], v[120:123]\n\t" \
  "v_mfma_f32_16x16x32_bf16 v[124:127], v[140:143], v[156:159], v[124:127]\n\t" \
  "s_waitcnt lgkmcnt(3)\n\t" \
  "v_mfma_f32_16x16x32_bf16 v[64:67], v[160:163], v[176:179], v[64:67]\n\t" \
  "v_mfma_f32_16x16x32_bf16 v[68:71], v[160:163], v[180:183], v[68:71]\n\t" \
  "v_mfma_f32_16x16x32_bf16 v[72:75], v[160:163], v[184:187], v[72:75]\n\t" \
  "v_mfma_f32_16x16x32_bf16 v[76:79], v[160:163], v[188:191], v[76:79]\n\t" \
  "s_waitcnt lgkmcnt(0)\n\t" \
  "v_mfma_f32_16x16x32_bf16 v[80:83], v[164:167], v[176:179], v[80:83]\n\t" \
  "v_mfma_f32_16x16x32_bf16 v[84:87], v[164:167], v[180:183], v[84:87]\n\t" \
  "v_mfma_f32_16x16x32_bf16 v[88:91], v[164:167], v[184:187], v[88:91]\n\t" \
  "s_barrier\n\t" \
  "v_mfma_f32_16x16x32_bf16 v[92:95], v[164:167], v[188:191], v[92:95]\n\t" \
  "ds_read_b128 v[128:131], %[a0] offset:32768\n\t" \
  "ds_read_b64_tr_b16 v[144:145], %[u0] offset:0\n\t" \
  "v_mfma_f32_16x16x32_bf16 v[96:99], v[168:171], v[176:179], v[96:99]\n\t" \
  "ds_read_b64_tr_b16 v[146:147], %[u1] offset:256\n\t" \
  "v_mfma_f32_16x16x32_bf16 v[100:103], v[168:171], v[180:183], v[100:103]\n\t" \
  "ds_read_b64_tr_b16 v[148:149], %[u2] offset:0\n\t" \
  "v_mfma_f32_16x16x32_bf16 v[104:107], v[168:171], v[184:187], v[104:107]\n\t" \
  "ds_read_b64_tr_b16 v[150:151], %[u3] offset:256\n\t" \
  "ds_read_b64_tr_b16 v[152:153], %[u0] offset:512\n\t" \
  "v_mfma_f32_16x16x32_bf16 v[108:111], v[168:171], v[188:191], v[108:111]\n\t" \
  "ds_read_b64_tr_b16 v[154:155], %[u1] offset:768\n\t" \
  "v_mfma_f32_16x16x32_bf16 v[112:115], v[172:175], v[176:179], v[112:115]\n\t" \
  "ds_read_b64_tr_b16 v[156:157], %[u2] offset:512\n\t" \
  "v_mfma_f32_16x16x32_bf16 v[116:119], v[172:175], v[180:183], v[116:119]\n\t" \
  "ds_read_b64_tr_b16 v[158:159], %[u3] offset:768\n\t" \
  "ds_read_b128 v[132:135], %[a0] offset:34816\n\t" \
  "v_mfma_f32_16x16x32_bf16 v[120:123], v[172:175], v[184:187], v[120:123]\n\t" \
  "ds_read_b128 v[136:139], %[a0] offset:36864\n\t" \
  "v_mfma_f32_16x16x32_bf16 v[124:127], v[172:175], v[188:191], v[124:127]\n\t" \
  "ds_read_b128 v[140:143], %[a0] offset:38912\n\t" \
  "s_waitcnt lgkmcnt(9)\n\t" \
  "v_mfma_f32_16x16x32_bf16 v[64:67], v[128:131], v[144:147], v[64:67]\n\t" \
  "ds_read_b128 v[160:163], %[a0] offset:33280\n\t" \
  "s_waitcnt lgkmcnt(8)\n\t" \
  "v_mfma_f32_16x16x32_bf16 v[68:71], v[128:131], v[148:151], v[68:71]\n\t" \
  "ds_read_b64_tr_b16 v[176:177], %[u0] offset:8192\n\t" \
  "s_waitcnt lgkmcnt(7)\n\t" \
  "v_mfma_f32_16x16x32_bf16 v[72:75], v[128:131], v[152:155], v[72:75]\n\t" \
  "ds_read_b64_tr_b16 v[178:179], %[u1] offset:8448\n\t" \
  "s_waitcnt lgkmcnt(6)\n\t" \
  "v_mfma_f32_16x16x32_bf16 v[76:79], v[128:131], v[156:159], v[76:79]\n\t" \
  "ds_read_b64_tr_b16 v[180:181], %[u2] offset:8192\n\t" \
  "s_waitcnt lgkmcnt(6)\n\t" \
  "v_mfma_f32_16x16x32_bf16 v[80:83], v[132:135], v[144:147], v[80:83]\n\t" \
  "ds_read_b64_tr_b16 v[182:183], %[u3] offset:8448\n\t" \
  "v_mfma_f32_16x16x32_bf16 v[84:87], v[132:135], v[148:151], v[84:87]\n\t" \
  "ds_read_b64_tr_b16 v[184:185], %[u0] offset:8704\n\t" \
  "v_mfma_f32_16x16x32_bf16 v[88:91], v[132:135], v[152:155], v[88:91]\n\t" \
  "ds_read_b64_tr_b16 v[186:187], %[u1] offset:8960\n\t" \
  "v_mfma_f32_16x16x32_bf16 v[92:95], v[132:135], v[156:159], v[92:95]\n\t" \
  "ds_read_b64_tr_b16 v[188:189], %[u2] offset:8704\n\t" \
  "s_waitcnt lgkmcnt(8)\n\t" \
  "v_mfma_f32_16x16x32_bf16 v[96:99], v[136:139], v[144:147], v[96:99]\n\t" \
  "ds_read_b64_tr_b16 v[190:191], %[u3] offset:8960\n\t" \
  "v_mfma_f32_16x16x32_bf16 v[100:103], v[136:139], v[148:151], v[100:103]\n\t" \
  "ds_read_b128 v[164:167], %[a0] offset:35328\n\t" \
  "v_mfma_f32_16x16x32_bf16 v[104:107], v[136:139], v[152:155], v[104:107]\n\t" \
  "ds_read_b128 v[168:171], %[a0] offset:37376\n\t" \
  "v_mfma_f32_16x16x32_bf16 v[108:111], v[136:139], v[156:159], v[108:111]\n\t" \
  "ds_read_b128 v[172:175], %[a0] offset:39424\n\t" \
  "v_mfma_f32_16x16x32_bf16 v[112:115], v[140:143], v[144:147], v[112:115]\n\t" \
  "v_mfma_f32_16x16x32_bf16 v[116:119], v[140:143], v[148:151], v[116:119]\n\t" \
  "v_mfma_f32_16x16x32_bf16 v[120:123], v[140:143], v[152:155], v[120:123]\n\t" \
  "v_mfma_f32_16x16x32_bf16 v[124:127], v[140:143], v[156:159], v[124:127]\n\t" \
  "s_waitcnt lgkmcnt(3)\n\t" \
  "v_mfma_f32_16x16x32_bf16 v[64:67], v[160:163], v[176:179], v[64:67]\n\t" \
  "v_mfma_f32_16x16x32_bf16 v[68:71], v[160:163], v[180:183], v[68:71]\n\t" \
  "v_mfma_f32_16x16x32_bf16 v[72:75], v[160:163], v[184:187], v[72:75]\n\t" \
  "v_mfma_f32_16x16x32_bf16 v[76:79], v[160:163], v[188:191], v[76:79]\n\t" \
  "s_waitcnt lgkmcnt(0)\n\t" \
  "v_mfma_f32_16x16x32_bf16 v[80:83], v[164:167], v[176:179], v[80:83]\n\t" \
  "v_mfma_f32_16x16x32_bf16 v[84:87], v[164:167], v[180:183], v[84:87]\n\t" \
  "v_mfma_f32_16x16x32_bf16 v[88:91], v[164:167], v[184:187], v[88:91]\n\t" \
  "s_barrier\n\t" \
  "v_mfma_f32_16x16x32_bf16 v[92:95], v[164:167], v[188:191], v[92:95]\n\t" \
  "ds_read_b128 v[140:143], %[a0] offset:55296\n\t" \
  "ds_read_b128 v[220:223], %[a0] offset:55808\n\t" \
  "ds_read_b64_tr_b16 v[144:145], %[u0] offset:16384\n\t" \
  "v_mfma_f32_16x16x32_bf16 v[96:99], v[168:171], v[176:179], v[96:99]\n\t" \
  "ds_read_b64_tr_b16 v[146:147], %[u1] offset:16640\n\t" \
  "ds_read_b64_tr_b16 v[224:225], %[u0] offset:24576\n\t" \
  "ds_read_b64_tr_b16 v[226:227], %[u1] offset:24832\n\t" \
  "v_mfma_f32_16x16x32_bf16 v[100:103], v[168:171], v[180:183], v[100:103]\n\t" \
  "ds_read_b128 v[128:131], %[a0] offset:49152\n\t" \
  "ds_read_b128 v[208:211], %[a0] offset:49664\n\t" \
  "v_mfma_f32_16x16x32_bf16 v[104:107], v[168:171], v[184:187], v[104:107]\n\t" \
  "ds_read_b128 v[132:135], %[a0] offset:51200\n\t" \
  "ds_read_b128 v[212:215], %[a0] offset:51712\n\t" \
  "ds_read_b128 v[136:139], %[a0] offset:53248\n\t" \
  "v_mfma_f32_16x16x32_bf16 v[108:111], v[168:171], v[188:191], v[108:111]\n\t" \
  "ds_read_b128 v[216:219], %[a0] offset:53760\n\t" \
  "ds_read_b64_tr_b16 v[152:153], %[u0] offset:16896\n\t" \
  "ds_read_b64_tr_b16 v[154:155], %[u1] offset:17152\n\t" \
  "v_mfma_f32_16x16x32_bf16 v[112:115], v[172:175], v[176:179], v[112:115]\n\t" \
  "ds_read_b64_tr_b16 v[232:233], %[u0] offset:25088\n\t" \
  "ds_read_b64_tr_b16 v[234:235], %[u1] offset:25344\n\t" \
  "v_mfma_f32_16x16x32_bf16 v[116:119], v[172:175], v[180:183], v[116:119]\n\t" \
  "ds_read_b64_tr_b16 v[148:149], %[u2] offset:16384\n\t" \
  "ds_read_b64_tr_b16 v[150:151], %[u3] offset:16640\n\t" \
  "ds_read_b64_tr_b16 v[228:229], %[u2] offset:24576\n\t" \
  "v_mfma_f32_16x16x32_bf16 v[120:123], v[172:175], v[184:187], v[120:123]\n\t" \
  "ds_read_b64_tr_b16 v[230:231], %[u3] offset:24832\n\t" \
  "ds_read_b64_tr_b16 v[156:157], %[u2] offset:16896\n\t" \
  "ds_read_b64_tr_b16 v[158:159], %[u3] offset:17152\n\t" \
  "v_mfma_f32_16x16x32_bf16 v[124:127], v[172:175], v[188:191], v[124:127]\n\t" \
  "ds_read_b64_tr_b16 v[236:237], %[u2] offset:25088\n\t" \
  "ds_read_b64_tr_b16 v[238:239], %[u3] offset:25344\n\t" \
  "s_waitcnt lgkmcnt(12)\n\t" \
  "v_mfma_f32_16x16x32_bf16 v[112:115], v[140:143], v[144:147], v[112:115]\n\t" \
  "v_mfma_f32_16x16x32_bf16 v[112:115], v[220:223], v[224:227], v[112:115]\n\t" \
  "s_nop 1\n\t" \
  "v_mfma_f32_16x16x32_bf16 v[64:67], v[128:131], v[144:147], v[64:67]\n\t" \
  "s_nop 1\n\t" \
  "v_mfma_f32_16x16x32_bf16 v[64:67], v[208:211], v[224:227], v[64:67]\n\t" \
  "s_nop 1\n\t" \
  "v_mfma_f32_16x16x32_bf16 v[80:83], v[132:135], v[144:147], v[80:83]\n\t" \
  "s_nop 1\n\t" \
  "v_mfma_f32_16x16x32_bf16 v[80:83], v[212:215], v[224:227], v[80:83]\n\t" \
  "v_cndmask_b32 v114, 0, v114, %[mk2]\n\t" \
  "v_cndmask_b32 v115, 0, v115, %[mk3]\n\t" \
  "v_mfma_f32_16x16x32_bf16 v[96:99], v[136:139], v[144:147], v[96:99]\n\t" \
  "v_pk_mul_f32 v[24:25], v[112:113], v[112:113]\n\t" \
  "v_pk_add_f32 v[16:17], v[112:113], v[114:115]\n\t" \
  "v_mfma_f32_16x16x32_bf16 v[96:99], v[216:219], v[224:227], v[96:99]\n\t" \
  "v_pk_fma_f32 v[24:25], v[114:115], v[114:115], v[24:25]\n\t" \
  "v_pk_add_f32 v[16:17], v[16:17], v[64:65]\n\t" \
  "s_waitcnt lgkmcnt(8)\n\t" \
  "v_mfma_f32_16x16x32_bf16 v[120:123], v[140:143], v[152:155], v[120:123]\n\t" \
  "v_pk_fma_f32 v[24:25], v[64:65], v[64:65], v[24:25]\n\t" \
  "v_pk_add_f32 v[16:17], v[16:17], v[66:67]\n\t" \
  "v_mfma_f32_16x16x32_bf16 v[120:123], v[220:223], v[232:235], v[120:123]\n\t" \
  "v_pk_fma_f32 v[24:25], v[66:67], v[66:67], v[24:25]\n\t" \
  "v_pk_add_f32 v[16:17], v[16:17], v[80:81]\n\t" \
  "v_mfma_f32_16x16x32_bf16 v[72:75], v[128:131], v[152:155], v[72:75]\n\t" \
  "v_pk_fma_f32 v[24:25], v[80:81], v[80:81], v[24:25]\n\t" \
  "v_pk_add_f32 v[16:17], v[16:17], v[82:83]\n\t" \
  "v_mfma_f32_16x16x32_bf16 v[72:75], v[208:211], v[232:235], v[72:75]\n\t" \
  "v_pk_fma_f32 v[24:25], v[82:83], v[82:83], v[24:25]\n\t" \
  "v_pk_add_f32 v[16:17], v[16:17], v[96:97]\n\t" \
  "v_mfma_f32_16x16x32_bf16 v[88:91], v[132:135], v[152:155], v[88:91]\n\t" \
  "v_pk_fma_f32 v[24:25], v[96:97], v[96:97], v[24:25]\n\t" \
  "v_pk_add_f32 v[16:17], v[16:17], v[98:99]\n\t" \
  "v_mfma_f32_16x16x32_bf16 v[88:91], v[212:215], v[232:235], v[88:91]\n\t" \
  "v_pk_fma_f32 v[24:25], v[98:99], v[98:99], v[24:25]\n\t" \
  "v_add_f32 v16, v16, v17\n\t" \
  "v_mfma_f32_16x16x32_bf16 v[104:107], v[136:139], v[152:155], v[104:107]\n\t" \
  "v_add_f32 v24, v24, v25\n\t" \
  "v_cndmask_b32 v122, 0, v122, %[mk2]\n\t" \
  "v_mfma_f32_16x16x32_bf16 v[104:107], v[216:219], v[232:235], v[104:107]\n\t" \
  "v_cndmask_b32 v123, 0, v123, %[mk3]\n\t" \
  "v_pk_mul_f32 v[28:29], v[120:121], v[120:121]\n\t" \
  "s_waitcnt lgkmcnt(4)\n\t" \
  "v_mfma_f32_16x16x32_bf16 v[116:119], v[140:143], v[148:151], v[116:119]\n\t" \
  "v_pk_add_f32 v[20:21], v[120:121], v[122:123]\n\t" \
  "v_pk_fma_f32 v[28:29], v[122:123], v[122:123], v[28:29]\n\t" \
  "v_mfma_f32_16x16x32_bf16 v[116:119], v[220:223], v[228:231], v[116:119]\n\t" \
  "v_pk_add_f32 v[20:21], v[20:21], v[72:73]\n\t" \
  "v_pk_fma_f32 v[28:29], v[72:73], v[72:73], v[28:29]\n\t" \
  "v_mfma_f32_16x16x32_bf16 v[68:71], v[128:131], v[148:151], v[68:71]\n\t" \
  "v_pk_add_f32 v[20:21], v[20:21], v[74:75]\n\t" \
  "v_pk_fma_f32 v[28:29], v[74:75], v[74:75], v[28:29]\n\t" \
  "v_mfma_f32_16x16x32_bf16 v[68:71], v[208:211], v[228:231], v[68:71]\n\t" \
  "v_pk_add_f32 v[20:21], v[20:21], v[88:89]\n\t" \
  "v_pk_fma_f32 v[28:29], v[88:89], v[88:89], v[28:29]\n\t" \
  "v_mfma_f32_16x16x32_bf16 v[84:87], v[132:135], v[148:151], v[84:87]\n\t" \
  "v_pk_add_f32 v[20:21], v[20:21], v[90:91]\n\t" \
  "v_pk_fma_f32 v[28:29], v[90:91], v[90:91], v[28:29]\n\t" \
  "v_mfma_f32_16x16x32_bf16 v[84:87], v[212:215], v[228:231], v[84:87]\n\t" \
  "v_pk_add_f32 v[20:21], v[20:21], v[104:105]\n\t" \
  "v_pk_fma_f32 v[28:29], v[104:105], v[104:105], v[28:29]\n\t" \
  "v_mfma_f32_16x16x32_bf16 v[100:103], v[136:139], v[148:151], v[100:103]\n\t" \
  "v_pk_add_f32 v[20:21], v[20:21], v[106:107]\n\t" \
  "v_pk_fma_f32 v[28:29], v[106:107], v[106:107], v[28:29]\n\t" \
  "v_mfma_f32_16x16x32_bf16 v[100:103], v[216:219], v[228:231], v[100:103]\n\t" \
  "v_add_f32 v20, v20, v21\n\t" \
  "v_add_f32 v28, v28, v29\n\t" \
  "s_waitcnt lgkmcnt(0)\n\t" \
  "v_mfma_f32_16x16x32_bf16 v[124:127], v[140:143], v[156:159], v[124:127]\n\t" \
  "s_nop 1\n\t" \
  "v_permlane32_swap_b32 v16, v20\n\t" \
  "v_mfma_f32_16x16x32_bf16 v[124:127], v[220:223], v[236:239], v[124:127]\n\t" \
  "v_permlane32_swap_b32 v24, v28\n\t" \
  "v_add_f32 v16, v16, v20\n\t" \
  "v_mfma_f32_16x16x32_bf16 v[76:79], v[128:131], v[156:159], v[76:79]\n\t" \
  "v_add_f32 v24, v24, v28\n\t" \
  "v_cndmask_b32 v118, 0, v118, %[mk2]\n\t" \
  "v_mfma_f32_16x16x32_bf16 v[76:79], v[208:211], v[236:239], v[76:79]\n\t" \
  "v_cndmask_b32 v119, 0, v119, %[mk3]\n\t" \
  "v_pk_mul_f32 v[26:27], v[116:117], v[116:117]\n\t" \
  "v_mfma_f32_16x16x32_bf16 v[92:95], v[132:135], v[156:159], v[92:95]\n\t" \
  "v_pk_add_f32 v[18:19], v[116:117], v[118:119]\n\t" \
  "v_pk_fma_f32 v[26:27], v[118:119], v[118:119], v[26:27]\n\t" \
  "v_mfma_f32_16x16x32_bf16 v[92:95], v[212:215], v[236:239], v[92:95]\n\t" \
  "v_pk_add_f32 v[18:19], v[18:19], v[68:69]\n\t" \
  "v_pk_fma_f32 v[26:27], v[68:69], v[68:69], v[26:27]\n\t" \
  "v_mfma_f32_16x16x32_bf16 v[108:111], v[136:139], v[156:159], v[108:111]\n\t" \
  "v_pk_add_f32 v[18:19], v[18:19], v[70:71]\n\t" \
  "v_pk_fma_f32 v[26:27], v[70:71], v[70:71], v[26:27]\n\t" \
  "v_mfma_f32_16x16x32_bf16 v[108:111], v[216:219], v[236:239], v[108:111]\n\t" \
  "v_pk_add_f32 v[18:19], v[18:19], v[84:85]\n\t" \
  "v_pk_fma_f32 v[26:27], v[84:85], v[84:85], v[26:27]\n\t" \
  "v_pk_add_f32 v[18:19], v[18:19], v[86:87]\n\t" \
  "v_pk_fma_f32 v[26:27], v[86:87], v[86:87], v[26:27]\n\t" \
  "v_pk_add_f32 v[18:19], v[18:19], v[100:101]\n\t" \
  "v_pk_fma_f32 v[26:27], v[100:101], v[100:101], v[26:27]\n\t" \
  "v_pk_add_f32 v[18:19], v[18:19], v[102:103]\n\t" \
  "v_pk_fma_f32 v[26:27], v[102:103], v[102:103], v[26:27]\n\t" \
  "v_add_f32 v18, v18, v19\n\t" \
  "v_add_f32 v26, v26, v27\n\t" \
  "v_cndmask_b32 v126, 0, v126, %[mk2]\n\t" \
  "v_cndmask_b32 v127, 0, v127, %[mk3]\n\t" \
  "v_pk_mul_f32 v[30:31], v[124:125], v[124:125]\n\t" \
  "v_pk_add_f32 v[22:23], v[124:125], v[126:127]\n\t" \
  "v_pk_fma_f32 v[30:31], v[126:127], v[126:127], v[30:31]\n\t" \
  "v_pk_add_f32 v[22:23], v[22:23], v[76:77]\n\t" \
  "v_pk_fma_f32 v[30:31], v[76:77], v[76:77], v[30:31]\n\t" \
  "v_pk_add_f32 v[22:23], v[22:23], v[78:79]\n\t" \
  "v_pk_fma_f32 v[30:31], v[78:79], v[78:79], v[30:31]\n\t" \
  "v_pk_add_f32 v[22:23], v[22:23], v[92:93]\n\t" \
  "v_pk_fma_f32 v[30:31], v[92:93], v[92:93], v[30:31]\n\t" \
  "v_pk_add_f32 v[22:23], v[22:23], v[94:95]\n\t" \
  "v_pk_fma_f32 v[30:31], v[94:95], v[94:95], v[30:31]\n\t" \
  "v_pk_add_f32 v[22:23], v[22:23], v[108:109]\n\t" \
  "v_pk_fma_f32 v[30:31], v[108:109], v[108:109], v[30:31]\n\t" \
  "v_pk_add_f32 v[22:23], v[22:23], v[110:111]\n\t" \
  "v_pk_fma_f32 v[30:31], v[110:111], v[110:111], v[30:31]\n\t" \
  "v_add_f32 v22, v22, v23\n\t" \
  "v_add_f32 v30, v30, v31\n\t" \
  "s_nop 1\n\t" \
  "v_permlane32_swap_b32 v18, v22\n\t" \
  "v_permlane32_swap_b32 v26, v30\n\t" \
  "v_add_f32 v18, v18, v22\n\t" \
  "v_add_f32 v26, v26, v30\n\t" \
  "s_nop 1\n\t" \
  "v_permlane16_swap_b32 v16, v18\n\t" \
  "v_permlane16_swap_b32 v24, v26\n\t" \
  "v_add_f32 v16, v16, v18\n\t" \
  "v_add_f32 v17, v24, v26\n\t" \
  "ds_write_b64 %[lp], v[16:17]\n\t" \
  "ds_write_b32 %[lf], %[zero]\n\t" \
  "s_waitcnt lgkmcnt(0)\n\t" \
  "s_barrier\n\t" \
  ""

constexpr int L_PART = LPB, L_AC = LPB + 2048, L_FL = LPB + 4096;
constexpr int HS = 544;
constexpr int L_H = 5 * SLOT;

#define CLOB16(a, b, c, d, e, f, g, h, i, j, k, l, m, n, o, p) "v" #a, "v" #b, "v" #c, "v" #d, "v" #e, "v" #f, "v" #g, "v" #h, "v" #i, "v" #j, "v" #k, "v" #l, "v" #m, "v" #n, "v" #o, "v" #p
typedef const __attribute__((address_space(4))) u64* kargp;
#define KARGS kargp kp = (kargp)__builtin_amdgcn_kernarg_segment_ptr(); asm volatile("" : "+s"(kp))
__global__ __launch_bounds__(512, 2) void k_fused(const unsigned short* x_, const unsigned short* w_, unsigned char* ws_, unsigned short* out_,
                                                  const unsigned short* gamma_, const unsigned short* beta_) {
  __shared__ __attribute__((aligned(16))) unsigned char smem[10 * SLOT];
  asm volatile("s_load_dword s98, %0, 0" :: "s"(__builtin_amdgcn_kernarg_segment_ptr()) : "s98");
  int g = blockIdx.x;
  const int tid = threadIdx.x, lane = tid & 63;
  u32 pf;
  { u64 pc0; asm volatile("s_getpc_b64 %0" : "=s"(pc0));
    asm volatile("s_mov_b64 exec, 1\n\tglobal_load_dword %0, %1, %2\n\ts_mov_b64 exec, -1" : "={v200}"(pf) : "v"(min(g >> 3, PF_SLICES - 1) * 512 + tid), "s"(pc0 & ~255ull)); }
  const int wave = __builtin_amdgcn_readfirstlane(tid >> 6);
  const u32 lds0 = (u32)(size_t)(LAS unsigned char*)smem;
  const bool spare = (g & 0xF3) == 0xF3;
#define INDICES KARGS; unsigned char* ws = (unsigned char*)kp[2]; unsigned short* out = (unsigned short*)kp[3]; int g_ = g; asm volatile("" : "+s"(g_)); const int half = (g_ >> 3) & 1, b = (g_ >> 2) & 1, m = (g_ >> 4) * 4 + (g_ & 3), grp = (g_ >> 2) & 3; \
    const __amdgpu_buffer_rsrc_t rsS = __builtin_amdgcn_make_buffer_rsrc((void*)ws, 0, WS_BYTES, 0x00020000); \
    const __amdgpu_buffer_rsrc_t rsO = __builtin_amdgcn_make_buffer_rsrc((void*)(out + (size_t)b * L_ * 256), 0, L_ * 512, 0x00020000); (void)rsS; (void)rsO; (void)grp; (void)m
  if (__builtin_expect(wave < 4 && !spare, 1)) {
    u32 nin, gg, bb, go;
    {
    KARGS;
    const u64 x = kp[0], w = kp[1];
    const int r8 = (lane >> 2) & 7, c16 = (((lane >> 5) << 2) | ((lane & 3) ^ (r8 >> 2))) << 4;
    const int vX0 = r8 * 1024 + c16, vX1 = (vX0 ^ 32) + 7168, vX2 = vX0 + 14336, vX3 = vX1 + 14336;
    const int vW0 = r8 * 512 + c16 + 1024, vW1 = vW0 - 896, vW2 = (vW0 ^ 32) + 2048, vW3 = vW2 - 896;
    const int xe = (((g >> 4) * 4 + (g & 3)) * (2 * TR) + ((g & 4) ? T_ : 0)) * 512 + wave * 32768;
    const int wo = wave * 8192 + ((g << 5) & 256);
    const u32 cx = CMAP ? (((g >> 3) ^ (g >> 6)) & 1) << 7 : 0;
    const u32 k0 = ((g << 3) + cx) & 0x180;
    const u32x4 VX = {(u32)vX0, (u32)vX1, (u32)vX2, (u32)vX3}, VW = {(u32)vW0, (u32)vW1, (u32)vW2, (u32)vW3};
    const u32 mb = lds0 + wave * 4096;
    asm volatile("" :: "{v[8:11]}"(VX), "{v[12:15]}"(VW), "s"(xe + k0), "s"(wo + (k0 << 8)), "s"(mb));
    SBAR;
    const u32x4 dX = {(u32)x, (u32)(x >> 32) & 0xffffu, 2 * T_ * 512, 0x00020000u};
    const u64 wp = w - 1024;
    const u32x4 dW = {(u32)wp, (u32)(wp >> 32) & 0xffffu, 768 * 512 + 1024, 0x00020000u};
    asm volatile("s_mov_b32 m0, %4\n\ts_nop 0\n\t"
                 "buffer_load_dwordx4 v8, %0, %2 offen lds\n\tbuffer_load_dwordx4 v9, %0, %2 offen offset:1024 lds\n\t"
                 "buffer_load_dwordx4 v10, %0, %2 offen offset:2048 lds\n\tbuffer_load_dwordx4 v11, %0, %2 offen offset:3072 lds\n\t"
                 "s_add_u32 m0, %4, %7\n\ts_nop 0\n\t"
                 "buffer_load_dwordx4 v12, %1, %3 offen lds\n\tbuffer_load_dwordx4 v13, %1, %3 offen offset:1024 lds\n\t"
                 "buffer_load_dwordx4 v14, %1, %3 offen offset:2048 lds\n\tbuffer_load_dwordx4 v15, %1, %3 offen offset:3072 lds"
                 :: "s"(dX), "s"(dW), "s"(xe + k0), "s"(wo + (k0 << 8)), "s"(mb), "{v[8:11]}"(VX), "{v[12:15]}"(VW), "i"(NXS * SLOT) : "memory", "scc");
    int g1 = g, l1 = lane; asm volatile("" : "+s"(g1), "+v"(l1), "+s"(kp));
    const u64 ws = kp[2], outp = kp[3], gamma = kp[4], beta = kp[5];
    const int m0 = (g1 >> 4) * 4 + (g1 & 3), grp0 = (g1 >> 2) & 3;
    const u32 cb = g1 << 3, k1 = (cb + (0x80 ^ cx)) & 0x180, k2 = (cb + (0x100 ^ cx)) & 0x180, k3 = (cb + (0x180 ^ cx)) & 0x180;
    const u32x8 P0 = {k0, k1, k2, k3, (u32)xe, (u32)xe + 512, mb, 0u};
    const u32x8 P1 = {k0 << 8, k1 << 8, k2 << 8, k3 << 8, (u32)wo, (u32)wo + 131072, (u32)wo + 262144, (u32)(wave == 0 ? 2 : 0)};
    const int vn = WS_NONCE + grp0 * 16384, va = WS_ARR + (grp0 * 64 + m0) * 256;
    const int vo = min(xe >> 1, 2 * L_ * 512 - 4);
    const int vc = ((g1 << 5) & 256) + wave * 64 + (l1 & 7) * 8 + (l1 >> 4) * 2;
    int so;
    asm volatile(STREAM : [so] "=&s"(so), [nin] "=&v"(nin), [gg] "=&v"(gg), [bb] "=&v"(bb), [go] "=&v"(go)
                 : "{s[40:47]}"(P0), "{s[48:55]}"(P1), "{v[8:11]}"(VX), "{v[12:15]}"(VW), [dx] "s"(dX), [dw] "s"(dW),
                   [ws] "s"(ws), [vn] "v"(vn), [va] "v"(va), [vc] "v"(vc), [vo] "v"(vo), [out] "s"(outp), [gam] "s"(gamma), [bet] "s"(beta), "{v200}"(pf) : "memory", "scc");
      }
    INDICES;
    u32 rel = __builtin_amdgcn_readfirstlane(go);
    if (__builtin_expect(rel - 1u > 1u, 0)) {
      _Pragma("unroll 1") for (u32 sp = 0; sp < SPIN_CAP; ++sp) {
        MEMF;
        if (wave == 0) {
          const u32 nn = __builtin_amdgcn_raw_buffer_load_b32(rsS, WS_NONCE + grp * 16384, 0, AUX_SC1);
          if (lane == 0 && nn != nin) { nin = nn; __builtin_amdgcn_raw_buffer_store_b32(nn, rsS, WS_ARR + (grp * 64 + m) * 256, 0, AUX_SC1); }
        }
        rel = __builtin_amdgcn_readfirstlane(__builtin_amdgcn_raw_buffer_load_b32(rsS, WS_GO + (grp * 64 + m) * 256, 0, AUX_SC1));
        if (rel - 1u <= 1u) break;
        __builtin_amdgcn_s_sleep(4);
      }
    }
    const int pbase = WS_PART + grp * 65536 + (rel & 2u) * 4096;
    const int poff = pbase + (m >> 4) * 16384 + (m & 15) * 512 + lane * 8, goff = pbase + (lane >> 3) * 512 + wave * 128 + (lane & 7) * 16;
    asm volatile("" :: "v"(poff), "v"(goff), "s"(rsS));
    MEMF; __builtin_amdgcn_s_barrier(); MEMF;
    if (wave == 0) {
      const f32x4 p = (*(const LAS f32x4*)(smem + L_PART + lane * 16) + *(const LAS f32x4*)(smem + L_PART + 1024 + lane * 16)) * (1.0f / TR);
      const u32 mb = pk_bf16((f32x2){p.x, p.z});
      const u32 u0 = __float_as_uint(fmaxf(p.y - p.x * p.x, 0.f)) + 0x8000u, u1 = __float_as_uint(fmaxf(p.w - p.z * p.z, 0.f)) + 0x8000u;
      const u32 m0 = mb & 0xffffu, m1 = mb >> 16;
      const u32x2 pub = {(__builtin_elementwise_sub_sat(u0, m0) & 0x7fff0000u) | m0, (__builtin_elementwise_sub_sat(u1, m1) & 0x7fff0000u) | m1};
      __builtin_amdgcn_raw_buffer_store_b64(pub, rsS, poff, 0, AUX_SC1);
    }
    if (wave) __builtin_amdgcn_s_sleep(GDELAY); else __builtin_amdgcn_s_sleep(GDELAY - 5);
    f32x2 S0, S1, Q0, Q1;
    const bool lastp = lane >= 56;
    _Pragma("unroll 1") for (u32 sp = 0;; ++sp) {
      MEMF;
      u32x4 v[8];
#pragma unroll
      for (int i = 0; i < 8; ++i) v[i] = __builtin_amdgcn_raw_buffer_load_b128(rsS, goff + (i == 7 && lastp ? 0x40000000 : 0), (i >> 1) * 16384 + (i & 1) * 4096, AUX_SC1);
      u32 bad = 0; S0 = (f32x2){0.f, 0.f}; S1 = S0; Q0 = S0; Q1 = S0;
#pragma unroll
      for (int i = 0; i < 8; ++i) {
        bad |= v[i].x | v[i].y | v[i].z | v[i].w;
        const f32x2 a = {__uint_as_float(v[i].x << 16), __uint_as_float(v[i].y << 16)}, c = {__uint_as_float(v[i].z << 16), __uint_as_float(v[i].w << 16)};
        S0 += a; S1 += c;
        Q0 = __builtin_elementwise_fma(a, a, Q0 + (f32x2){__uint_as_float(v[i].x), __uint_as_float(v[i].y)});
        Q1 = __builtin_elementwise_fma(c, c, Q1 + (f32x2){__uint_as_float(v[i].z), __uint_as_float(v[i].w)});
      }
      if (__all((int)bad >= 0) || sp > SPIN_CAP) break;
    }
    const float S = add_ror<8>(add_x16(add_x32(S0.x, S1.x), add_x32(S0.y, S1.y))), Q = add_ror<8>(add_x16(add_x32(Q0.x, Q1.x), add_x32(Q0.y, Q1.y)));
    const float mu = S * ((float)TR / L_), var = fmaxf(Q * ((float)TR / L_) - mu * mu, 0.f);
    const float a = __uint_as_float(gg << 16) * __builtin_amdgcn_rsqf(var + 1e-5f);
    *(LAS float*)(smem + L_AC + wave * 128 + (lane & 7) * 16 + (lane >> 4) * 4 + (lane & 8) * 64) = (lane & 8) ? __uint_as_float(bb << 16) - mu * a : a;
    asm volatile("s_waitcnt lgkmcnt(0)\n\ts_barrier" ::: "memory");
    } else if (__builtin_expect(!spare, 1)) {
    INDICES;
    const int wq = wave - 4, wr = wq >> 1, wc = wq & 1;
    const int co = wq == 2 ? WS_PART + grp * 65536 + (m >> 4) * 16384 + (m & 15) * 512 + (lane >> 5) * 8192 + (lane & 31) * 16 : WS_GO + (grp * 64 + m) * 256;
    const u64 cm = __ballot(wq == 2 || (wq == 3 && lane == 0));
    f32x16 c0, c1, c2, c3;
    const int n = lane & 15, q4 = lane >> 4, tq = n >> 2, tp = lane & 3;
    const int j0 = wr * 64 + 2 * n - (n < 4 ? 0 : (n < 12 ? 7 : 16)), j1 = j0 + 1;
    const u32 as0 = lds0 + (j0 >> 3) * 1024 + (j0 & 7) * 64 + ((q4 ^ ((j0 >> 2) & 3)) << 4), as1 = lds0 + (j1 >> 3) * 1024 + (j1 & 7) * 64 + ((q4 ^ ((j1 >> 2) & 3)) << 4);
    const u32 w0 = lds0 + NXS * SLOT + q4 * 2048 + wc * 1024 + tq * 64 + (((tp >> 1) ^ (2 * (q4 & 1))) << 4) + 8 * (tp & 1), w1 = w0 ^ 16, w2 = w0 ^ 32, w3 = w0 ^ 48;
    const int rb = q4 == 0 ? 0 : (q4 == 1 ? 1 : (q4 == 2 ? 9 : 8));
    const int nrow = (m == GS - 1 ? L_ - (GS - 1) * TR : TR) - wr * 64;
    const u64 mk2 = __ballot(52 + rb < nrow), mk3 = __ballot(54 + rb < nrow);
    asm volatile(KLOOP16
                 : "=&{v[64:79]}"(c0), "=&{v[80:95]}"(c1), "=&{v[96:111]}"(c2), "=&{v[112:127]}"(c3)
                 : "{v200}"(pf), [a0] "v"(as0), [a1] "v"(as1), [b0] "v"(as0 + 4 * SLOT), [b1] "v"(as1 + 4 * SLOT), [w0] "v"(w0), [w1] "v"(w1), [w2] "v"(w2), [w3] "v"(w3),
                   [u0] "v"(w0 + 4 * SLOT), [u1] "v"(w1 + 4 * SLOT), [u2] "v"(w2 + 4 * SLOT), [u3] "v"(w3 + 4 * SLOT), [mk2] "s"(mk2), [mk3] "s"(mk3), [lp] "v"(lds0 + L_PART + wr * 1024 + (wc * 64 + lane) * 8), [lf] "v"(lds0 + L_FL + wq * 4), [zero] "v"(0),
                   [co] "v"(co), [cm] "s"(cm), [cv] "s"(wq == 2 ? ~0u : 0u), [rs] "s"(rsS)
                 : "memory", "vcc", "scc", CLOB16(16, 17, 18, 19, 20, 21, 22, 23, 24, 25, 26, 27, 28, 29, 30, 31),
                   CLOB16(128, 129, 130, 131, 132, 133, 134, 135, 136, 137, 138, 139, 140, 141, 142, 143), CLOB16(144, 145, 146, 147, 148, 149, 150, 151, 152, 153, 154, 155, 156, 157, 158, 159),
                   CLOB16(160, 161, 162, 163, 164, 165, 166, 167, 168, 169, 170, 171, 172, 173, 174, 175), CLOB16(176, 177, 178, 179, 180, 181, 182, 183, 184, 185, 186, 187, 188, 189, 190, 191),
                   CLOB16(208, 209, 210, 211, 212, 213, 214, 215, 216, 217, 218, 219, 220, 221, 222, 223), CLOB16(224, 225, 226, 227, 228, 229, 230, 231, 232, 233, 234, 235, 236, 237, 238, 239));
    const f32x16 acc[4] = {c0, c1, c2, c3};
    { const int hb = L_H + (wr * 64 + rb) * HS + (wc * 8 + (n >> 3)) * 16 + ((n >> 2) & 1) * 256 + (n & 3) * 4;
#pragma unroll
      for (int i = 0; i < 4; ++i)
#pragma unroll
        for (int j = 0; j < 4; ++j)
#pragma unroll
          for (int e = 0; e < 4; ++e) *(LAS float*)(smem + hb + (i * 16 + 2 * e) * HS + j * 32) = acc[i][4 * j + e]; }
    asm volatile("s_waitcnt lgkmcnt(0)" ::: "memory");
    *(volatile LAS u32*)(smem + L_FL + wq * 4) = 1u;
  } else {
    INDICES;
    if (wave == 0) {
      const int sg = grp;
      const int cal = WS_PART + grp * 65536 + 3 * 16384 + 15 * 512;
      __builtin_amdgcn_raw_buffer_store_b128((u32x4){0u, 0u, 0u, 0u}, rsS, cal + (lane >> 5) * 8192 + (lane & 31) * 16, 0, AUX_SC1);
      if (lane < GS) __builtin_amdgcn_raw_buffer_store_b32(0u, rsS, WS_ARR + (sg * 64 + lane) * 256, 0, AUX_SC1);
      const u32 old = __builtin_amdgcn_raw_buffer_load_b32(rsS, WS_NONCE + sg * 16384, 0, AUX_SC1);
      asm volatile("s_waitcnt vmcnt(0)" :: "{v200}"(pf) : "memory");
      const u32 nn = old + 1u ? old + 1u : 1u;
      if (lane == 0) __builtin_amdgcn_raw_buffer_store_b32(nn, rsS, WS_NONCE + sg * 16384, 0, AUX_SC1);
      u32 best0 = ~0u, best1 = ~0u;
      _Pragma("unroll 1") for (int i = 0; i < 8; ++i) {
        u64 c0, c1; u32 v;
        asm volatile("s_waitcnt vmcnt(0)\n\ts_memtime %0\n\ts_waitcnt lgkmcnt(0)\n\tbuffer_load_dword %2, %3, %4, 0 offen sc1\n\ts_waitcnt vmcnt(0)\n\ts_memtime %1\n\ts_waitcnt lgkmcnt(0)"
                     : "=&s"(c0), "=&s"(c1), "=&v"(v) : "v"(cal + (i & 1) * 8192 + (i >> 1) * 128), "s"(rsS) : "memory");
        const u32 d = (u32)(c1 - c0);
        if (i & 1) best1 = min(best1, d); else best0 = min(best0, d);
      }
      const u32 rel = best1 < best0 ? 2u : 1u;
      _Pragma("unroll 1") for (u32 sp = 0; sp < SPIN_CAP; ++sp) {
        MEMF;
        const u32 a = __builtin_amdgcn_raw_buffer_load_b32(rsS, WS_ARR + (sg * 64 + lane) * 256, 0, AUX_SC1);
        if (__all(a == nn || lane >= GS)) break;
        __builtin_amdgcn_s_sleep(2);
      }
      __builtin_amdgcn_raw_buffer_store_b32(rel, rsS, WS_GO + (sg * 64 + lane) * 256, 0, AUX_SC1);
    }
    return;
    }
  INDICES;
  f32x4 hv[4][2];
  _Pragma("unroll 1") for (u32 sp = 0; sp < SPIN_CAP; ++sp) {
    const u32x4 d = *(volatile const LAS u32x4*)(smem + L_FL);
    if (__builtin_amdgcn_readfirstlane(d.x & d.y & d.z & d.w)) break;
    __builtin_amdgcn_s_sleep(1);
  }
  MEMF;
#pragma unroll
  for (int i = 0; i < 4; ++i)
#pragma unroll
    for (int e = 0; e < 2; ++e) hv[i][e] = *(const LAS f32x4*)(smem + L_H + ((tid >> 4) + 32 * i) * HS + e * 256 + (tid & 15) * 16);
  if (wave >= 4) __builtin_amdgcn_s_barrier();
  MEMF;
  {
    f32x4 ac[4];
#pragma unroll
    for (int e = 0; e < 4; ++e) ac[e] = *(const LAS f32x4*)(smem + L_AC + (tid & 15) * 32 + (e & 1) * 16 + (e >> 1) * 512);
    const int ov = (tid >> 4) * 512 + half * 256 + (tid & 15) * 16;
#pragma unroll
    for (int i = 0; i < 4; ++i) {
      u32x4 o;
#pragma unroll
      for (int e = 0; e < 4; ++e) {
        const f32x2 v = {hv[i][e >> 1][(e & 1) * 2], hv[i][e >> 1][(e & 1) * 2 + 1]};
        const f32x2 a = {ac[e >> 1][(e & 1) * 2], ac[e >> 1][(e & 1) * 2 + 1]}, c = {ac[2 + (e >> 1)][(e & 1) * 2], ac[2 + (e >> 1)][(e & 1) * 2 + 1]};
        o[e] = pk_bf16(gelu2(__builtin_elementwise_fma(v, a, c)));
      }
      if (i < 3 || tid < 496) __builtin_amdgcn_raw_buffer_store_b128(o, rsO, ov, (m * TR + 32 * i) * 512, AUXO);
      SBAR;
    }
  }
}

extern "C" void kernel_launch(void* const* d_in, const int* in_sizes, int n_in, void* d_out, int out_size, void* d_ws, size_t ws_size, hipStream_t stream) {
  k_fused<<<256, 512, 0, stream>>>((const unsigned short*)d_in[0], (const unsigned short*)d_in[1], (unsigned char*)d_ws, (unsigned short*)d_out,
                                   (const unsigned short*)d_in[2], (const unsigned short*)d_in[3]);
}
```
